```python
import torch

def _softmax(x, axis=-1):
    return torch.softmax(x, dim=axis)
def _gelu(x):
    return torch.nn.functional.gelu(x, approximate="tanh")
def _silu(x):
    return torch.nn.functional.silu(x)
def _sigmoid(x):
    return torch.sigmoid(x)
def _layernorm(x, axis=-1, eps=1e-5):
    m = x.mean(dim=axis, keepdim=True)
    v = x.var(dim=axis, keepdim=True, unbiased=False)
    return (x - m) / torch.sqrt(v + eps)
def _scatter_add(tgt, idx, src, axis=0):
    return tgt.clone().index_add_(axis, idx, src)
def _segment_sum(src, seg, n):
    out = torch.zeros((n,) + src.shape[1:], dtype=src.dtype)
    return out.index_add_(0, seg, src)
def _pad(x, pw):
    flat = tuple(v for pair in reversed(pw) for v in pair)
    return torch.nn.functional.pad(x, flat)

kernel_name = 'gated_mix__gqa_block_cached__swiglu_ffn__decode'

def setup_inputs():

    _w = (lambda a: (lambda t: t.to(torch.bfloat16) if t.is_floating_point() else t)(torch.as_tensor(a)))
    def _A_setup():
        B, Sq, D = 2, 1, 576
        Hq, Hkv, Sk, Dh = 32, 16, 4096, 128
        assert Hq % Hkv == 0, (Hq, Hkv)
        return {
            "x": _w((lambda *s: torch.randn(*s, dtype=torch.bfloat16) * 0.05)(B, Sq, D)),
            "k_cache": _w((lambda *s: torch.randn(*s, dtype=torch.bfloat16) * 6.4)(B, Hkv, Sk, Dh)),
            "v_cache": _w((lambda *s: torch.randn(*s, dtype=torch.bfloat16) * 0.8)(B, Hkv, Sk, Dh)),
            "wq": _w((lambda *s: torch.randn(*s, dtype=torch.bfloat16) * 6.4)(D, Hq * Dh) * (D ** -0.5)),
            "wo": _w((lambda *s: torch.randn(*s, dtype=torch.bfloat16) * 0.8)(Hq * Dh, D) * ((Hq * Dh) ** -0.5)),
        }
    _A_in = _A_setup()

    def _B_setup():
        B, S, D, F = 2, 1, 576, 4608
        return {
            "x": _w((lambda *s: torch.randn(*s, dtype=torch.bfloat16) * 0.05)(B, S, D)),
            "w_gate": _w((lambda *s: torch.randn(*s, dtype=torch.bfloat16) * 0.2)(D, F)),
            "w_up": _w((lambda *s: torch.randn(*s, dtype=torch.bfloat16) * 0.2)(D, F)),
            "w_down": _w((lambda *s: torch.randn(*s, dtype=torch.bfloat16) * 0.2)(F, D)),
        }
    _B_in = _B_setup()

    return {
        "A_x": _A_in["x"],
        "A_k_cache": _A_in["k_cache"],
        "A_v_cache": _A_in["v_cache"],
        "A_wq": _A_in["wq"],
        "A_wo": _A_in["wo"],
        "B_x": _B_in["x"],
        "B_w_gate": _B_in["w_gate"],
        "B_w_up": _B_in["w_up"],
        "B_w_down": _B_in["w_down"],
        "gate_w": _w((lambda *s: torch.randn(*s, dtype=torch.bfloat16) * 0.05)(576) * 16.0),
    }

def reference(A_x, A_k_cache, A_v_cache, A_wq, A_wo, B_x, B_w_gate, B_w_up, B_w_down, gate_w):
    def _A_ref(x, k_cache, v_cache, wq, wo):
        B, Sq, D = x.shape
        Hq, Dh = 32, 128
        Hkv = k_cache.shape[1]
        rep = Hq // Hkv
        q = (lambda x, *d: x.permute(*d))((x @ wq).reshape(B, Sq, Hq, Dh), 0, 2, 1, 3)
        k = (lambda x, n, axis: torch.repeat_interleave(x, n, dim=axis))(k_cache, rep, axis=1)
        v = (lambda x, n, axis: torch.repeat_interleave(x, n, dim=axis))(v_cache, rep, axis=1)
        scores = torch.einsum("bhqd,bhkd->bhqk", q, k) * (Dh ** -0.5)
        p = _softmax(scores, axis=-1)
        o = torch.einsum("bhqk,bhkd->bhqd", p, v)
        return (lambda x, *d: x.permute(*d))(o, 0, 2, 1, 3).reshape(B, Sq, Hq * Dh) @ wo

    def _B_ref(x, w_gate, w_up, w_down):
        g = _silu(x @ w_gate)
        u = x @ w_up
        return (g * u) @ w_down

    x = A_x
    g = _sigmoid((x * gate_w).sum(-1, keepdims=True))
    a = _A_ref(x, A_k_cache, A_v_cache, A_wq, A_wo)
    b = _B_ref(x, B_w_gate, B_w_up, B_w_down)
    return g * a + (1 - g) * b
```

```cpp
#include <hip/hip_runtime.h>
#include <hip/hip_bf16.h>
#include <stdint.h>

typedef unsigned short u16;
typedef unsigned v4u __attribute__((ext_vector_type(4)));

constexpr int NB = 2, DM = 576, HQ = 32, HKV = 16, SK = 4096, DH = 128, FF = 4608;
constexpr int NSPLIT = 8;

__device__ __forceinline__ float bf2f(u16 b) { return __builtin_bit_cast(float, (unsigned)b << 16); }
__device__ __forceinline__ u16 f2bf(float f) {
  unsigned u = __builtin_bit_cast(unsigned, f);
  u += 0x7FFFu + ((u >> 16) & 1u);
  return (u16)(u >> 16);
}

__global__ void __launch_bounds__(64) k1(const u16* x, const u16* wq, const u16* wg, const u16* wu, float* q, float* h) {
  __shared__ float xs[2 * DM];
  for (int i = threadIdx.x; i < 2 * DM; i += 64) xs[i] = bf2f(x[i]);
  __syncthreads();
  int col = blockIdx.x * 64 + threadIdx.x;
  if (col < HQ * DH) {
    float a0 = 0, a1 = 0;
    for (int k = 0; k < DM; ++k) { float w = bf2f(wq[(size_t)k * HQ * DH + col]); a0 += xs[k] * w; a1 += xs[DM + k] * w; }
    q[col] = bf2f(f2bf(a0)); q[HQ * DH + col] = bf2f(f2bf(a1));
  } else {
    col -= HQ * DH;
    float g0 = 0, g1 = 0, u0 = 0, u1 = 0;
    for (int k = 0; k < DM; ++k) {
      float w = bf2f(wg[(size_t)k * FF + col]); g0 += xs[k] * w; g1 += xs[DM + k] * w;
      float v = bf2f(wu[(size_t)k * FF + col]); u0 += xs[k] * v; u1 += xs[DM + k] * v;
    }
    h[col] = g0 / (1.f + __expf(-g0)) * u0;
    h[FF + col] = g1 / (1.f + __expf(-g1)) * u1;
  }
}

__global__ void __launch_bounds__(256) k2(const u16* kc, const u16* vc, const float* q, float* pml, float* po) {
  const int s = blockIdx.x, kvh = blockIdx.y, b = blockIdx.z;
  constexpr int T = SK / NSPLIT;
  __shared__ float qs[2 * DH];
  __shared__ float sc[2 * T];
  __shared__ float red[2][4];
  __shared__ float oacc[4][2][DH];
  const int tid = threadIdx.x;
  qs[tid] = q[(size_t)b * HQ * DH + kvh * 2 * DH + tid];
  __syncthreads();
  const u16* kb = kc + ((size_t)(b * HKV + kvh) * SK + s * T) * DH;
  const u16* vb = vc + ((size_t)(b * HKV + kvh) * SK + s * T) * DH;
  const float scale = 0.08838834764831845f * 1.4426950408889634f;
  for (int t = tid; t < T; t += 256) {
    float a0 = 0, a1 = 0;
    for (int d = 0; d < DH; d += 8) {
      v4u kv = *(const v4u*)(kb + (size_t)t * DH + d);
      for (int j = 0; j < 4; ++j) {
        float lo = __builtin_bit_cast(float, kv[j] << 16), hi = __builtin_bit_cast(float, kv[j] & 0xffff0000u);
        a0 += lo * qs[d + 2 * j] + hi * qs[d + 2 * j + 1];
        a1 += lo * qs[DH + d + 2 * j] + hi * qs[DH + d + 2 * j + 1];
      }
    }
    sc[t] = a0 * scale; sc[T + t] = a1 * scale;
  }
  __syncthreads();
  float m0 = -INFINITY, m1 = -INFINITY;
  for (int t = 0; t < T; ++t) { m0 = fmaxf(m0, sc[t]); m1 = fmaxf(m1, sc[T + t]); }
  __syncthreads();
  for (int t = tid; t < T; t += 256) { sc[t] = exp2f(sc[t] - m0); sc[T + t] = exp2f(sc[T + t] - m1); }
  __syncthreads();
  const int w = tid >> 6, lane = tid & 63;
  float o00 = 0, o01 = 0, o10 = 0, o11 = 0, l0 = 0, l1 = 0;
  for (int t = w * (T / 4); t < (w + 1) * (T / 4); ++t) {
    unsigned vv = *(const unsigned*)(vb + (size_t)t * DH + 2 * lane);
    float lo = __builtin_bit_cast(float, vv << 16), hi = __builtin_bit_cast(float, vv & 0xffff0000u);
    float p0 = sc[t], p1 = sc[T + t];
    o00 += p0 * lo; o01 += p0 * hi; o10 += p1 * lo; o11 += p1 * hi; l0 += p0; l1 += p1;
  }
  oacc[w][0][2 * lane] = o00; oacc[w][0][2 * lane + 1] = o01; oacc[w][1][2 * lane] = o10; oacc[w][1][2 * lane + 1] = o11;
  if (lane == 0) { red[0][w] = l0; red[1][w] = l1; }
  __syncthreads();
  const int hh = tid >> 7, d = tid & 127;
  float o = oacc[0][hh][d] + oacc[1][hh][d] + oacc[2][hh][d] + oacc[3][hh][d];
  size_t row = ((size_t)b * HQ + kvh * 2 + hh) * NSPLIT + s;
  po[row * DH + d] = o;
  if (d == 0) { pml[row * 2] = hh ? m1 : m0; pml[row * 2 + 1] = red[hh][0] + red[hh][1] + red[hh][2] + red[hh][3]; }
}

__global__ void __launch_bounds__(320) k3(const u16* x, const u16* gw, const u16* wo, const u16* wd, const float* h, const float* pml, const float* po, float* part) {
  __shared__ float act[2][34];
  __shared__ float gs[2];
  const int j = blockIdx.x, tid = threadIdx.x;
  if (tid < 32) {
    int b = tid >> 4, r = 16 * j + (tid & 15), hd = r >> 7, d = r & 127;
    size_t row = ((size_t)b * HQ + hd) * NSPLIT;
    float M = -INFINITY;
    for (int s = 0; s < NSPLIT; ++s) M = fmaxf(M, pml[(row + s) * 2]);
    float L = 0, o = 0;
    for (int s = 0; s < NSPLIT; ++s) { float w = exp2f(pml[(row + s) * 2] - M); L += w * pml[(row + s) * 2 + 1]; o += w * po[(row + s) * DH + d]; }
    act[b][tid & 15] = o / L;
  } else if (tid < 32 + 36) {
    int i = tid - 32, b = i / 18, r = i % 18;
    act[b][16 + r] = h[b * FF + 18 * j + r];
  } else if (tid >= 128 && tid < 256) {
    int b = (tid - 128) >> 6, lane = tid & 63;
    float s = 0;
    for (int k = lane; k < DM; k += 64) s += bf2f(x[b * DM + k]) * bf2f(gw[k]);
    for (int o = 32; o; o >>= 1) s += __shfl_xor(s, o);
    if (lane == 0) gs[b] = 1.f / (1.f + __expf(-s));
  }
  __syncthreads();
  if (tid < 288) {
    float a00 = 0, a01 = 0, a10 = 0, a11 = 0, f00 = 0, f01 = 0, f10 = 0, f11 = 0;
    for (int r = 0; r < 16; ++r) {
      unsigned w = *(const unsigned*)(wo + (size_t)(16 * j + r) * DM + 2 * tid);
      float lo = __builtin_bit_cast(float, w << 16), hi = __builtin_bit_cast(float, w & 0xffff0000u);
      a00 += act[0][r] * lo; a01 += act[0][r] * hi; a10 += act[1][r] * lo; a11 += act[1][r] * hi;
    }
    for (int r = 0; r < 18; ++r) {
      unsigned w = *(const unsigned*)(wd + (size_t)(18 * j + r) * DM + 2 * tid);
      float lo = __builtin_bit_cast(float, w << 16), hi = __builtin_bit_cast(float, w & 0xffff0000u);
      f00 += act[0][16 + r] * lo; f01 += act[0][16 + r] * hi; f10 += act[1][16 + r] * lo; f11 += act[1][16 + r] * hi;
    }
    float g0 = gs[0], g1 = gs[1];
    float* pp = part + (size_t)j * 2 * DM;
    pp[2 * tid] = g0 * a00 + (1.f - g0) * f00; pp[2 * tid + 1] = g0 * a01 + (1.f - g0) * f01;
    pp[DM + 2 * tid] = g1 * a10 + (1.f - g1) * f10; pp[DM + 2 * tid + 1] = g1 * a11 + (1.f - g1) * f11;
  }
}

__global__ void __launch_bounds__(256) k4(const float* part, u16* out) {
  __shared__ float red[16][17];
  const int o = blockIdx.x * 16 + (threadIdx.x & 15), g = threadIdx.x >> 4;
  float s = 0;
  for (int p = g * 16; p < g * 16 + 16; ++p) s += part[(size_t)p * 2 * DM + o];
  red[g][threadIdx.x & 15] = s;
  __syncthreads();
  if (threadIdx.x < 16) {
    float t = 0;
    for (int i = 0; i < 16; ++i) t += red[i][threadIdx.x];
    out[o] = f2bf(t);
  }
}

extern "C" void kernel_launch(void* const* d_in, const int* in_sizes, int n_in, void* d_out, int out_size, void* d_ws, size_t ws_size, hipStream_t stream) {
  const u16* x = (const u16*)d_in[0];
  const u16* kc = (const u16*)d_in[1];
  const u16* vc = (const u16*)d_in[2];
  const u16* wq = (const u16*)d_in[3];
  const u16* wo = (const u16*)d_in[4];
  const u16* wg = (const u16*)d_in[6];
  const u16* wu = (const u16*)d_in[7];
  const u16* wd = (const u16*)d_in[8];
  const u16* gw = (const u16*)d_in[9];
  float* ws = (float*)d_ws;
  float* q = ws;
  float* h = q + 2 * 4096;
  float* pml = h + 2 * FF;
  float* po = pml + 2 * HQ * NSPLIT * 2;
  float* part = po + 2 * HQ * NSPLIT * DH;
  k1<<<(HQ * DH + FF) / 64, 64, 0, stream>>>(x, wq, wg, wu, q, h);
  k2<<<dim3(NSPLIT, HKV, NB), 256, 0, stream>>>(kc, vc, q, pml, po);
  k3<<<256, 320, 0, stream>>>(x, gw, wo, wd, h, pml, po, part);
  k4<<<72, 256, 0, stream>>>(part, (u16*)d_out);
}
```

```cpp
#include <hip/hip_runtime.h>
#include <stdint.h>

typedef unsigned short u16;
typedef unsigned long long u64;
typedef unsigned v4u __attribute__((ext_vector_type(4)));
typedef unsigned v3u __attribute__((ext_vector_type(3)));
typedef unsigned v2u __attribute__((ext_vector_type(2)));
typedef float v4f __attribute__((ext_vector_type(4)));
typedef float v2f __attribute__((ext_vector_type(2)));
typedef __bf16 bf2 __attribute__((ext_vector_type(2)));
typedef __bf16 bf8 __attribute__((ext_vector_type(8)));
typedef __amdgpu_buffer_rsrc_t rsrc_t;

constexpr int DM = 576, SK = 4096, FF = 4608;
constexpr int NQ = 19;
constexpr int AUX_NT = 2, AUX_SC1 = 16;
constexpr unsigned OOR = 0x80000000u;
constexpr int SPIN_MAX = 1 << 16;
#define PF 16
#define F7 125
#define F6 122

#define QSPIN SPIN_MAX
constexpr unsigned WS_EPOCH = 0;
constexpr unsigned WS_QB = 4096;
constexpr unsigned WS_O1 = WS_QB + 16 * 16384;
constexpr unsigned WS_ML = WS_O1 + 16 * 16 * 2048;
constexpr unsigned WS_HB = WS_ML + 16 * 2 * 256;
constexpr unsigned WS_FB = WS_HB + 6 * FF * 16;
constexpr unsigned WS_END = WS_FB + 144 * 16384;

#define STAMP(k) do { } while (0)

__device__ __forceinline__ float as_f(unsigned u) { return __builtin_bit_cast(float, u); }
__device__ __forceinline__ unsigned as_u(float f) { return __builtin_bit_cast(unsigned, f); }
__device__ __forceinline__ bf2 as_bf2(unsigned u) { return __builtin_bit_cast(bf2, u); }
__device__ __forceinline__ bf8 as_bf8(v4u u) { return __builtin_bit_cast(bf8, u); }
__device__ __forceinline__ unsigned pk_bf16(float a, float b) { v2f p = {a, b}; return __builtin_bit_cast(unsigned, __builtin_convertvector(p, bf2)); }
__device__ __forceinline__ float rnd_bf16(float a) { return as_f(pk_bf16(a, a) & 0xffff0000u); }
template <int CTRL> __device__ __forceinline__ float dpp(float v) {
  return as_f((unsigned)__builtin_amdgcn_update_dpp(0, (int)as_u(v), CTRL, 0xf, 0xf, true));
}
constexpr int DPP_ROR8 = 0x128, DPP_ROR4 = 0x124, DPP_HMIRROR = 0x141, DPP_X1 = 0xB1, DPP_X2 = 0x4E;
__device__ __forceinline__ float sum8(float t) {
  t += dpp<DPP_HMIRROR>(t); t += dpp<DPP_X1>(t); t += dpp<DPP_X2>(t); return t;
}
__device__ __forceinline__ float max8(float t) {
  t = fmaxf(t, dpp<DPP_HMIRROR>(t)); t = fmaxf(t, dpp<DPP_X1>(t)); t = fmaxf(t, dpp<DPP_X2>(t)); return t;
}
__device__ __forceinline__ float sum_rows(float t) {
  auto a = __builtin_amdgcn_permlane16_swap(as_u(t), as_u(t), false, false);
  unsigned a0 = a[0], a1 = a[1];
  t = as_f(a0) + as_f(a1);
  auto b = __builtin_amdgcn_permlane32_swap(as_u(t), as_u(t), false, false);
  unsigned b0 = b[0], b1 = b[1];
  return as_f(b0) + as_f(b1);
}
__device__ __forceinline__ float max_rows(float t) {
  auto a = __builtin_amdgcn_permlane16_swap(as_u(t), as_u(t), false, false);
  unsigned a0 = a[0], a1 = a[1];
  t = fmaxf(as_f(a0), as_f(a1));
  auto b = __builtin_amdgcn_permlane32_swap(as_u(t), as_u(t), false, false);
  unsigned b0 = b[0], b1 = b[1];
  return fmaxf(as_f(b0), as_f(b1));
}
__device__ __forceinline__ rsrc_t mk_rsrc(const void* p, unsigned bytes) {
  return __builtin_amdgcn_make_buffer_rsrc(const_cast<void*>(p), 0, bytes, 0x00020000);
}
__device__ __forceinline__ v4u ld_nt(rsrc_t r, unsigned voff, unsigned soff) { return __builtin_amdgcn_raw_buffer_load_b128(r, voff, soff, AUX_NT); }
__device__ __forceinline__ v4u ld_sc(rsrc_t r, unsigned voff, unsigned soff) { return __builtin_amdgcn_raw_buffer_load_b128(r, voff, soff, AUX_SC1); }
__device__ __forceinline__ void st_sc(v4u v, rsrc_t r, unsigned voff, unsigned soff) { __builtin_amdgcn_raw_buffer_store_b128(v, r, voff, soff, AUX_SC1); }
__device__ __forceinline__ v4f mfma(v4u a, v4u b, v4f c) {
  return __builtin_amdgcn_mfma_f32_16x16x32_bf16(as_bf8(a), as_bf8(b), c, 0, 0, 0);
}
struct Tag { unsigned t32, t8; };
__device__ __forceinline__ v4u gran2(float a, float b, Tag t) {
  v4u g;
  g.x = ((as_u(a) + 0x80u) & 0xffffff00u) | t.t8; g.y = t.t32;
  g.z = ((as_u(b) + 0x80u) & 0xffffff00u) | t.t8; g.w = t.t32;
  return g;
}
__device__ __forceinline__ unsigned gbad(v4u g, Tag t) {
  unsigned x = g.x, y = g.y, z = g.z, w = g.w;
  return (((x ^ t.t8) | (z ^ t.t8)) & 0xffu) | (y ^ t.t32) | (w ^ t.t32);
}
__device__ __forceinline__ float gv0(v4u g) { unsigned x = g.x; return as_f(x & 0xffffff00u); }
__device__ __forceinline__ float gv1(v4u g) { unsigned z = g.z; return as_f(z & 0xffffff00u); }
#define SB() __builtin_amdgcn_sched_barrier(0)
#define CBAR() asm volatile("" ::: "memory")
__device__ __forceinline__ void wg_barrier() {
  asm volatile("s_waitcnt lgkmcnt(0)" ::: "memory");
  __builtin_amdgcn_s_barrier();
  asm volatile("" ::: "memory");
}
__device__ __forceinline__ unsigned dup_lo(unsigned p) { return __builtin_amdgcn_perm(p, p, 0x01000100u); }
__device__ __forceinline__ unsigned dup_hi(unsigned p) { return __builtin_amdgcn_perm(p, p, 0x03020302u); }

struct __attribute__((aligned(16))) Lds {
  float ow[8][256];
  float fu[8][256];
  v4u tr[8][72];
  unsigned qs[2][64];
  unsigned tabo[2][16];
  unsigned tabh[2][24];
  float mw[2][8], lw[2][8];
  float red[8][4][2];
  float gsig[2];
  unsigned qflag, fcnt;
};

__global__ void __launch_bounds__(512, 2)
fused_decode(const u16* __restrict__ x, const u16* __restrict__ kc, const u16* __restrict__ vc, const u16* __restrict__ wq,
             const u16* __restrict__ wo, const u16* __restrict__ wg, const u16* __restrict__ wu, const u16* __restrict__ wd,
             const u16* __restrict__ gw, u16* __restrict__ out, unsigned char* ws) {
  __shared__ Lds L;
  const int bid = blockIdx.x, tid = threadIdx.x, l = tid & 63;
  const int w = __builtin_amdgcn_readfirstlane(tid >> 6);
  const int xc = bid & 7, slot = bid >> 3, p4 = slot >> 3;
  const int kvh = 2 * xc + (slot >> 4), b = p4 & 1, s = slot & 7, j = slot & 15;
  const int nFp = p4 == 0 ? 6 : 7;
  const bool isF = s < nFp;
  const int funit = xc * 27 + (p4 == 0 ? s : 7 * p4 - 1 + s);
  const int fcb = funit / 3, fks = funit - 3 * fcb;
  const int g = l >> 4, c = l & 15, e = l & 7, hb = (l >> 3) & 1;
  STAMP(0);
  const u64 ep = *(const __attribute__((address_space(4))) u64*)(ws + WS_EPOCH) + 1ull;
  const Tag tag = {(unsigned)ep, (unsigned)(ep >> 32) & 0xffu};
  const rsrc_t rws = mk_rsrc(ws, WS_END);
  const rsrc_t rx = mk_rsrc(x, 2 * DM * 2);

  int q0, nq;
  {
    const int nF = p4 == 0 ? F6 : F7, nN = p4 == 0 ? (1024 - 6 * F6) / 2 : 1024 - 7 * F7;
    const int wg0 = isF ? s * nF : nFp * nF + (s - nFp) * nN;
    const int per = isF ? PF : NQ;
    const int first = (isF ? nF : nN) - 7 * per;
    q0 = wg0 + (w == 0 ? 0 : first + (w - 1) * per);
    nq = w == 0 ? first : per;
  }
  const size_t kvoff = ((size_t)(b * 16 + kvh) * SK + 4 * q0) * 128;
  const rsrc_t rk = mk_rsrc(kc + kvoff, nq * 1024);
  const rsrc_t rv = mk_rsrc(vc + kvoff, nq * 1024);

  v4u dm;
  {
    const unsigned hm = (e & 1) ? 0xffff0000u : 0x0000ffffu;
    dm.x = (e >> 1) == 0 ? hm : 0u; dm.y = (e >> 1) == 1 ? hm : 0u; dm.z = (e >> 1) == 2 ? hm : 0u; dm.w = (e >> 1) == 3 ? hm : 0u;
  }
  if (tid == 0) { L.qflag = 0; L.fcnt = 0; }
  const int cb = j & 3, kq = j >> 2;
  unsigned xa[2][9];
  v4u wr[18];
  unsigned gq[3][5];
  if (w == 0) {
    __builtin_amdgcn_s_setprio(3);
    const rsrc_t rq = mk_rsrc(wq + (size_t)kq * 144 * 4096 + kvh * 256 + cb * 64, 144 * 8192);
    const unsigned vo = (72 * hb + 18 * g) * 8192 + e * 16;
#pragma unroll
    for (int r = 0; r < 2; ++r) {
      const unsigned xo = (hb * DM + kq * 144 + 72 * r + 18 * g) * 2;
      v4u t0 = __builtin_amdgcn_raw_buffer_load_b128(rx, xo, 0, 0), t1 = __builtin_amdgcn_raw_buffer_load_b128(rx, xo + 16, 0, 0);
      xa[r][0] = t0.x; xa[r][1] = t0.y; xa[r][2] = t0.z; xa[r][3] = t0.w;
      xa[r][4] = t1.x; xa[r][5] = t1.y; xa[r][6] = t1.z; xa[r][7] = t1.w;
      xa[r][8] = __builtin_amdgcn_raw_buffer_load_b32(rx, xo + 32, 0, 0);
    }
#pragma unroll
    for (int i = 0; i < 18; ++i) wr[i] = ld_nt(rq, vo, i * 8192);
    SB();
  }
  wg_barrier();
  if (w == 2) {
    const rsrc_t rg = mk_rsrc(gw, DM * 2);
#pragma unroll
    for (int i = 0; i < 5; ++i) {
      const unsigned o = (i * 64 + l) * 4;
      gq[0][i] = __builtin_amdgcn_raw_buffer_load_b32(rg, o, 0, 0);
      gq[1][i] = __builtin_amdgcn_raw_buffer_load_b32(rx, o, 0, 0);
      gq[2][i] = __builtin_amdgcn_raw_buffer_load_b32(rx, o + DM * 2, 0, 0);
    }
    SB();
  }
  v4u kr[NQ];
#pragma unroll
  for (int i = 0; i < NQ; ++i) kr[i] = ld_nt(rk, l * 16 + (i >> 2) * 4096 + (i & 3) * 1024, 0);
  SB();
  if (w == 0) {
    v4f d0 = {0, 0, 0, 0}, d1 = {0, 0, 0, 0};
#pragma unroll
    for (int i = 0; i < 18; ++i) {
      const unsigned p0 = (i & 1) ? dup_hi(xa[0][i >> 1]) : dup_lo(xa[0][i >> 1]);
      const unsigned p1 = (i & 1) ? dup_hi(xa[1][i >> 1]) : dup_lo(xa[1][i >> 1]);
      d0 = mfma(dm & p0, wr[i], d0);
      d1 = mfma(dm & p1, wr[i], d1);
    }
    float r0 = hb ? d1.x : d0.x, r1 = hb ? d1.y : d0.y, r2 = hb ? d1.z : d0.z, r3 = hb ? d1.w : d0.w;
    r0 += dpp<DPP_ROR8>(r0); r1 += dpp<DPP_ROR8>(r1); r2 += dpp<DPP_ROR8>(r2); r3 += dpp<DPP_ROR8>(r3);
    st_sc(gran2(hb ? r2 : r0, hb ? r3 : r1, tag), rws, WS_QB + ((kvh * 2 + (l >> 5)) * 4 + kq) * 2048 + cb * 512 + (l & 31) * 16, 0);
    STAMP(1);
  }
  v4u fw[6];
  v3u xf;
  if (isF) {
    xf = __builtin_amdgcn_raw_buffer_load_b96(rx, (hb * DM + fks * 192 + 24 * w + 6 * g) * 2, 0, 0);
    const u16* fp = (hb ? wu : wg) + (size_t)(fks * 192 + 24 * w + 6 * g) * FF + fcb * 64 + e * 8;
#pragma unroll
    for (int i = 0; i < 6; ++i) fw[i] = __builtin_nontemporal_load((const v4u*)(fp + (size_t)i * FF));
    SB();
  }
  v4u vr[NQ];
#pragma unroll
  for (int i = 0; i < NQ; ++i) vr[i] = ld_nt(rv, l * 16 + (i >> 2) * 4096 + (i & 3) * 1024, 0);
  SB();
  if (w == 0) {
    const unsigned qoff = WS_QB + (kvh * 2 + b) * 8192 + l * 16;
    v4u qa[8];
#pragma nounroll
    for (int spin = 0; spin < QSPIN; ++spin) {
      CBAR();
#pragma unroll
      for (int i = 0; i < 8; ++i) qa[i] = ld_sc(rws, qoff, i * 1024);
      unsigned bad = 0;
#pragma unroll
      for (int i = 0; i < 8; ++i) bad |= gbad(qa[i], tag);
      if (__builtin_amdgcn_ballot_w64(bad != 0) == 0) break;
      __builtin_amdgcn_s_sleep(2);
    }
    const int pi = (l >> 5) * 32 + 4 * e + 2 * (g & 1) + hb;
#pragma unroll
    for (int h = 0; h < 2; ++h) {
      const float a0 = (gv0(qa[h]) + gv0(qa[2 + h])) + (gv0(qa[4 + h]) + gv0(qa[6 + h]));
      const float a1 = (gv1(qa[h]) + gv1(qa[2 + h])) + (gv1(qa[4 + h]) + gv1(qa[6 + h]));
      L.qs[h][pi] = pk_bf16(a0, a1);
    }
    asm volatile("s_waitcnt lgkmcnt(0)" ::: "memory");
    __hip_atomic_store(&L.qflag, 1u, __ATOMIC_RELAXED, __HIP_MEMORY_SCOPE_WORKGROUP);
    CBAR();
    STAMP(2);
  }
  if (w == 2) {
    float s0 = 0.f, s1 = 0.f;
#pragma unroll
    for (int i = 0; i < 5; ++i) {
      s0 = __builtin_amdgcn_fdot2_f32_bf16(as_bf2(gq[0][i]), as_bf2(gq[1][i]), s0, false);
      s1 = __builtin_amdgcn_fdot2_f32_bf16(as_bf2(gq[0][i]), as_bf2(gq[2][i]), s1, false);
    }
    s0 = sum8(s0); s0 += dpp<DPP_ROR8>(s0); s0 = sum_rows(s0);
    s1 = sum8(s1); s1 += dpp<DPP_ROR8>(s1); s1 = sum_rows(s1);
    if (l < 2) { const float sv = l ? s1 : s0; L.gsig[l] = 1.f / (1.f + __expf(-sv)); }
  }
  if (isF) {
    v4f d = {0, 0, 0, 0};
    const unsigned x0 = xf.x, x1 = xf.y, x2 = xf.z;
    d = mfma(dm & dup_lo(x0), fw[0], d); d = mfma(dm & dup_hi(x0), fw[1], d);
    d = mfma(dm & dup_lo(x1), fw[2], d); d = mfma(dm & dup_hi(x1), fw[3], d);
    d = mfma(dm & dup_lo(x2), fw[4], d); d = mfma(dm & dup_hi(x2), fw[5], d);
    *(v4f*)&L.fu[w][c * 16 + 4 * g] = d;
    asm volatile("s_waitcnt lgkmcnt(0)" ::: "memory");
    unsigned cnt = 0;
    if (l == 0) cnt = __hip_atomic_fetch_add(&L.fcnt, 1u, __ATOMIC_RELAXED, __HIP_MEMORY_SCOPE_WORKGROUP);
    cnt = __builtin_amdgcn_readfirstlane(cnt);
    CBAR();
    if (cnt == 7) {
      const int ch = l >> 3;
      float ga[2] = {0.f, 0.f}, up[2] = {0.f, 0.f};
#pragma unroll
      for (int bb = 0; bb < 2; ++bb)
#pragma unroll
        for (int ww = 0; ww < 8; ++ww) { ga[bb] += L.fu[ww][ch * 16 + bb * 8 + e]; up[bb] += L.fu[ww][(8 + ch) * 16 + bb * 8 + e]; }
      const unsigned off = WS_HB + (fks * 2 * FF + fcb * 64 + l) * 16;
      st_sc(gran2(ga[0], ga[1], tag), rws, off, 0);
      st_sc(gran2(up[0], up[1], tag), rws, off, FF * 16);
    }
  }
#pragma nounroll
  for (int spin = 0; spin < SPIN_MAX * 16; ++spin) {
    CBAR();
    if (__builtin_amdgcn_readfirstlane(__hip_atomic_load(&L.qflag, __ATOMIC_RELAXED, __HIP_MEMORY_SCOPE_WORKGROUP))) break;
    __builtin_amdgcn_s_sleep(1);
  }
  CBAR();
  STAMP(3);
  const v4u qA = *(const v4u*)&L.qs[hb][4 * c], qB = *(const v4u*)&L.qs[hb ^ 1][4 * c];
  float sc[NQ];
  float mx = -INFINITY;
#pragma unroll
  for (int i = 0; i < NQ; ++i) {
    const v4u kk = kr[i];
    const unsigned k0 = kk.x, k1 = kk.y, k2 = kk.z, k3 = kk.w;
    float t = __builtin_amdgcn_fdot2_f32_bf16(as_bf2(k0), as_bf2(qA.x), 0.f, false);
    float u = __builtin_amdgcn_fdot2_f32_bf16(as_bf2(k0), as_bf2(qB.x), 0.f, false);
    t = __builtin_amdgcn_fdot2_f32_bf16(as_bf2(k1), as_bf2(qA.y), t, false);
    u = __builtin_amdgcn_fdot2_f32_bf16(as_bf2(k1), as_bf2(qB.y), u, false);
    t = __builtin_amdgcn_fdot2_f32_bf16(as_bf2(k2), as_bf2(qA.z), t, false);
    u = __builtin_amdgcn_fdot2_f32_bf16(as_bf2(k2), as_bf2(qB.z), u, false);
    t = __builtin_amdgcn_fdot2_f32_bf16(as_bf2(k3), as_bf2(qA.w), t, false);
    u = __builtin_amdgcn_fdot2_f32_bf16(as_bf2(k3), as_bf2(qB.w), u, false);
    t += dpp<DPP_ROR8>(u);
    t = sum8(t);
    t = i < nq ? t : -INFINITY;
    sc[i] = t;
    mx = fmaxf(mx, t);
  }
  STAMP(4);
  const float SCL = 0.08838834764831845f * 1.4426950408889634f;
  mx = max_rows(mx);
  const float mneg = -mx * SCL;
  float lsum = 0.f;
  v4f oacc = {0, 0, 0, 0};
#pragma unroll
  for (int i = 0; i < NQ; ++i) {
    const float p = __builtin_amdgcn_exp2f(fmaf(sc[i], SCL, mneg));
    lsum += p;
    oacc = mfma(dm & pk_bf16(p, p), vr[i], oacc);
  }
  SB();
  v4u tw[9];
  {
    const rsrc_t ro = mk_rsrc(wo + (size_t)(kvh * 256 + 16 * j) * DM, 16 * DM * 2);
    const rsrc_t rd = mk_rsrc(wd + (size_t)bid * 18 * DM, 18 * DM * 2);
    const unsigned cofs = c < 9 ? (9 * w + c) * 16 : OOR;
#pragma unroll
    for (int i = 0; i < 4; ++i) tw[i] = ld_nt(ro, cofs + 4 * g * 1152, i * 1152);
#pragma unroll
    for (int i = 0; i < 5; ++i) tw[4 + i] = ld_nt(rd, cofs + 5 * g * 1152 + i * 1152, 0);
    SB();
  }
  lsum = sum_rows(lsum);
  *(v4f*)&L.ow[w][(l >> 5) * 128 + 8 * c + 4 * (g & 1)] = oacc;
  if (l < 16 && e == 0) { L.mw[hb][w] = mx * SCL; L.lw[hb][w] = lsum; }
  STAMP(5);
  wg_barrier();
  if (w == 2 || w == 3) {
    const int hd = w - 2;
    const v4f ma = *(const v4f*)&L.mw[hd][0], mb = *(const v4f*)&L.mw[hd][4];
    const v4f la = *(const v4f*)&L.lw[hd][0], lb = *(const v4f*)&L.lw[hd][4];
    float M = fmaxf(fmaxf(fmaxf(ma.x, ma.y), fmaxf(ma.z, ma.w)), fmaxf(fmaxf(mb.x, mb.y), fmaxf(mb.z, mb.w)));
    M = as_f(as_u(M) & 0xffffff00u);
    float f[8] = {ma.x, ma.y, ma.z, ma.w, mb.x, mb.y, mb.z, mb.w};
    const float lv[8] = {la.x, la.y, la.z, la.w, lb.x, lb.y, lb.z, lb.w};
    float o0 = 0.f, o1 = 0.f, ls = 0.f;
#pragma unroll
    for (int ww = 0; ww < 8; ++ww) {
      f[ww] = __builtin_amdgcn_exp2f(f[ww] - M);
      const v2f ov = *(const v2f*)&L.ow[ww][hd * 128 + 2 * l];
      o0 = fmaf(f[ww], ov.x, o0); o1 = fmaf(f[ww], ov.y, o1); ls = fmaf(f[ww], lv[ww], ls);
    }
    st_sc(gran2(o0, o1, tag), rws, WS_O1 + ((kvh * 16 + hd * 8 + (l >> 3)) * 16 + j) * 128 + (l & 7) * 16, 0);
    if (l == 0) {
      v4u gm = gran2(0.f, ls, tag);
      gm.x = as_u(M) | tag.t8;
      st_sc(gm, rws, WS_ML + ((kvh * 2 + hd) * 16 + j) * 16, 0);
    }
    STAMP(6);
  } else if (w == 0) {
    const unsigned off = WS_O1 + ((kvh * 16 + j) * 16 + c) * 128 + g * 32;
    const unsigned offm = WS_ML + ((kvh * 2 + (j >> 3)) * 16 + c) * 16;
    v4u a0, a1, am;
#pragma nounroll
    for (int spin = 0; spin < SPIN_MAX; ++spin) {
      CBAR();
      a0 = ld_sc(rws, off, 0); a1 = ld_sc(rws, off + 16, 0); am = ld_sc(rws, offm, 0);
      const unsigned bad = gbad(a0, tag) | gbad(a1, tag) | gbad(am, tag);
      if (__builtin_amdgcn_ballot_w64(bad != 0) == 0) break;
      __builtin_amdgcn_s_sleep(1);
    }
    const float m = gv0(am), M = max8(m);
    const float f = __builtin_amdgcn_exp2f(m - M);
    const float Ls = sum8(f * gv1(am));
    const float inv = 1.f / Ls;
    const float v0 = sum8(f * gv0(a0)) * inv, v1 = sum8(f * gv1(a0)) * inv, v2 = sum8(f * gv0(a1)) * inv, v3 = sum8(f * gv1(a1)) * inv;
    if (e == 0) {
      v4u t = {pk_bf16(v0, v0), pk_bf16(v1, v1), pk_bf16(v2, v2), pk_bf16(v3, v3)};
      *(v4u*)&L.tabo[hb][4 * g] = t;
    }
    STAMP(6);
  } else if (w == 1) {
    const unsigned off = l < 18 ? WS_HB + (bid * 18 + l) * 16 : OOR;
    v4u a[6];
#pragma nounroll
    for (int spin = 0; spin < SPIN_MAX; ++spin) {
      CBAR();
      unsigned bad = 0;
#pragma unroll
      for (int i = 0; i < 6; ++i) a[i] = ld_sc(rws, off, i * FF * 16);
#pragma unroll
      for (int i = 0; i < 6; ++i) bad |= gbad(a[i], tag);
      if (__builtin_amdgcn_ballot_w64(bad != 0 && l < 18) == 0) break;
      __builtin_amdgcn_s_sleep(1);
    }
    float hv[2];
#pragma unroll
    for (int bb = 0; bb < 2; ++bb) {
      const float ga = rnd_bf16(bb ? (gv1(a[0]) + gv1(a[2])) + gv1(a[4]) : (gv0(a[0]) + gv0(a[2])) + gv0(a[4]));
      const float up = rnd_bf16(bb ? (gv1(a[1]) + gv1(a[3])) + gv1(a[5]) : (gv0(a[1]) + gv0(a[3])) + gv0(a[5]));
      const float si = rnd_bf16(ga / (1.f + __expf(-ga)));
      hv[bb] = si * up;
    }
    if (l < 20) {
      const int ti = (l / 5) * 6 + l % 5;
      L.tabh[0][ti] = pk_bf16(hv[0], hv[0]); L.tabh[1][ti] = pk_bf16(hv[1], hv[1]);
    }
    STAMP(6);
  }
  wg_barrier();
  STAMP(7);
  {
    const v4u to = *(const v4u*)&L.tabo[hb][4 * g];
    const v2u h0 = *(const v2u*)&L.tabh[hb][6 * g], h1 = *(const v2u*)&L.tabh[hb][6 * g + 2];
    const unsigned h2 = L.tabh[hb][6 * g + 4];
    const float gs = L.gsig[l >> 5];
    v4f da = {0, 0, 0, 0}, db = {0, 0, 0, 0};
    da = mfma(dm & to.x, tw[0], da); db = mfma(dm & h0.x, tw[4], db);
    da = mfma(dm & to.y, tw[1], da); db = mfma(dm & h0.y, tw[5], db);
    da = mfma(dm & to.z, tw[2], da); db = mfma(dm & h1.x, tw[6], db);
    da = mfma(dm & to.w, tw[3], da); db = mfma(dm & h1.y, tw[7], db);
    db = mfma(dm & h2, tw[8], db);
    const float g1 = 1.f - gs;
    const float r0 = gs * da.x + g1 * db.x, r1 = gs * da.y + g1 * db.y, r2 = gs * da.z + g1 * db.z, r3 = gs * da.w + g1 * db.w;
    if (c < 9) { L.tr[w][c * 8 + g] = gran2(r0, r1, tag); L.tr[w][c * 8 + 4 + g] = gran2(r2, r3, tag); }
    const v4u t0 = L.tr[w][l], t1 = L.tr[w][64 + e];
    const unsigned off = WS_FB + (w * 18 + (l >> 2)) * 16384 + bid * 64 + (l & 3) * 16;
    st_sc(t0, rws, off, 0);
    st_sc(t1, rws, l < 8 ? off : OOR, 16 * 16384);
  }
  STAMP(8);
  if (bid < 144) {
    const unsigned off = WS_FB + bid * 16384 + w * 2048 + l * 16;
    v4u a0, a1;
#pragma nounroll
    for (int spin = 0; spin < SPIN_MAX; ++spin) {
      CBAR();
      a0 = ld_sc(rws, off, 0); a1 = ld_sc(rws, off + 1024, 0);
      const unsigned bad = gbad(a0, tag) | gbad(a1, tag);
      if (__builtin_amdgcn_ballot_w64(bad != 0) == 0) break;
      __builtin_amdgcn_s_sleep(1);
    }
    float v0 = gv0(a0) + gv0(a1), v1 = gv1(a0) + gv1(a1);
    v0 += dpp<DPP_ROR4>(v0); v1 += dpp<DPP_ROR4>(v1);
    v0 += dpp<DPP_ROR8>(v0); v1 += dpp<DPP_ROR8>(v1);
    v0 = sum_rows(v0); v1 = sum_rows(v1);
    if (l < 4) { v2f t = {v0, v1}; *(v2f*)&L.red[w][l][0] = t; }
    wg_barrier();
    if (tid < 4) {
      float t0 = 0.f, t1 = 0.f;
#pragma unroll
      for (int ww = 0; ww < 8; ++ww) { const v2f t = *(const v2f*)&L.red[ww][tid][0]; t0 += t.x; t1 += t.y; }
      const int uw = bid / 18, uc = (bid >> 1) % 9, rp = bid & 1;
      *(unsigned*)(out + (tid >> 1) * DM + 72 * uw + 8 * uc + 4 * (tid & 1) + 2 * rp) = pk_bf16(t0, t1);
      if (bid == 0 && tid == 0) *(u64*)(ws + WS_EPOCH) = ep;
    }
  }
  STAMP(9);
}

extern "C" void kernel_launch(void* const* d_in, const int* in_sizes, int n_in, void* d_out, int out_size, void* d_ws, size_t ws_size, hipStream_t stream) {
  fused_decode<<<256, 512, 0, stream>>>((const u16*)d_in[0], (const u16*)d_in[1], (const u16*)d_in[2], (const u16*)d_in[3], (const u16*)d_in[4],
                                        (const u16*)d_in[6], (const u16*)d_in[7], (const u16*)d_in[8], (const u16*)d_in[9], (u16*)d_out,
                                        (unsigned char*)d_ws);
}
```

```cpp
#include <hip/hip_runtime.h>
#include <stdint.h>

typedef unsigned short u16;
typedef unsigned long long u64;
typedef unsigned v16u __attribute__((ext_vector_type(16)));
typedef unsigned v4u __attribute__((ext_vector_type(4)));
typedef unsigned v3u __attribute__((ext_vector_type(3)));
typedef unsigned v2u __attribute__((ext_vector_type(2)));
typedef float v4f __attribute__((ext_vector_type(4)));
typedef float v2f __attribute__((ext_vector_type(2)));
typedef __bf16 bf2 __attribute__((ext_vector_type(2)));
typedef __bf16 bf8 __attribute__((ext_vector_type(8)));
typedef __amdgpu_buffer_rsrc_t rsrc_t;
#define GAS __attribute__((address_space(1)))

constexpr int DM = 576, SK = 4096, FF = 4608;
constexpr int NP = 12, NPMIN = 9, NOPT = NP - NPMIN;
constexpr int AUX_NT = 2, AUX_SC1 = 16;
constexpr unsigned OOR = 0x80000000u;
#define ZI
constexpr int SPIN_MAX = 1 << 16;
#define NTA 62
#define NTB 64
#define NTC 62
#define N5A 72
#define N6A 58
#define N6B 57
#define N6C 70
#define KF 4
#define TQ 450
#define TH 850
#define PD 3
#define PGAP 6
#define RD 3
#define RGAP 6
#define RSLOW 16
constexpr bool okq(int n) { return n >= 6 * NPMIN && n <= 6 * NP; }
static_assert(okq(NTA) && okq(NTB) && okq(NTC) && okq(N5A) && okq(N6A) && okq(N6B) && okq(N6C) && okq(512 - 5 * NTA - N5A - N6A) &&
              okq(512 - 6 * NTB - N6B) && okq(512 - 6 * NTC - N6C), "token counts out of range");
constexpr unsigned WS_EPOCH = 0;
constexpr unsigned WS_G = 1024;
constexpr unsigned WS_QB = 4096;
constexpr unsigned WS_O1 = WS_QB + 16 * 16384;
constexpr unsigned WS_ML = WS_O1 + 16 * 16 * 2048;
constexpr unsigned WS_HB = WS_ML + 16 * 2 * 256;
constexpr unsigned WS_FB = WS_HB + 6 * FF * 16;
constexpr unsigned WS_END = WS_FB + 9 * 64 * 1024;

#define STAMP(k) do { } while (0)

__device__ __forceinline__ float as_f(unsigned u) { return __builtin_bit_cast(float, u); }
__device__ __forceinline__ unsigned as_u(float f) { return __builtin_bit_cast(unsigned, f); }
__device__ __forceinline__ bf2 as_bf2(unsigned u) { return __builtin_bit_cast(bf2, u); }
__device__ __forceinline__ bf8 as_bf8(v4u u) { return __builtin_bit_cast(bf8, u); }
__device__ __forceinline__ unsigned pk_bf16(float a, float b) { v2f p = {a, b}; return __builtin_bit_cast(unsigned, __builtin_convertvector(p, bf2)); }
__device__ __forceinline__ float rnd_bf16(float a) { return as_f(pk_bf16(a, a) & 0xffff0000u); }
template <int CTRL> __device__ __forceinline__ float dpp(float v) {
  return as_f((unsigned)__builtin_amdgcn_update_dpp(0, (int)as_u(v), CTRL, 0xf, 0xf, true));
}
constexpr int DPP_ROR8 = 0x128, DPP_ROR4 = 0x124, DPP_HMIRROR = 0x141, DPP_X1 = 0xB1, DPP_X2 = 0x4E;
__device__ __forceinline__ float sum8(float t) {
  t += dpp<DPP_HMIRROR>(t); t += dpp<DPP_X1>(t); t += dpp<DPP_X2>(t); return t;
}
__device__ __forceinline__ float max8(float t) {
  t = fmaxf(t, dpp<DPP_HMIRROR>(t)); t = fmaxf(t, dpp<DPP_X1>(t)); t = fmaxf(t, dpp<DPP_X2>(t)); return t;
}
__device__ __forceinline__ float sum_rows(float t) {
  auto a = __builtin_amdgcn_permlane16_swap(as_u(t), as_u(t), false, false);
  unsigned a0 = a[0], a1 = a[1];
  t = as_f(a0) + as_f(a1);
  auto b = __builtin_amdgcn_permlane32_swap(as_u(t), as_u(t), false, false);
  unsigned b0 = b[0], b1 = b[1];
  return as_f(b0) + as_f(b1);
}
__device__ __forceinline__ float max_rows(float t) {
  auto a = __builtin_amdgcn_permlane16_swap(as_u(t), as_u(t), false, false);
  unsigned a0 = a[0], a1 = a[1];
  t = fmaxf(as_f(a0), as_f(a1));
  auto b = __builtin_amdgcn_permlane32_swap(as_u(t), as_u(t), false, false);
  unsigned b0 = b[0], b1 = b[1];
  return fmaxf(as_f(b0), as_f(b1));
}
__device__ __forceinline__ rsrc_t mk_rsrc(const void* p, unsigned bytes) {
  return __builtin_amdgcn_make_buffer_rsrc(const_cast<void*>(p), 0, bytes, 0x00020000);
}
__device__ __forceinline__ v4u ld_nt(rsrc_t r, unsigned voff, unsigned soff) { return __builtin_amdgcn_raw_buffer_load_b128(r, voff, soff, AUX_NT); }
__device__ __forceinline__ v4u ld_sc(rsrc_t r, unsigned voff, unsigned soff) { return __builtin_amdgcn_raw_buffer_load_b128(r, voff, soff, AUX_SC1); }
#define AUXG AUX_SC1
__device__ __forceinline__ v4u ld_g(rsrc_t r, unsigned voff, unsigned soff) { return __builtin_amdgcn_raw_buffer_load_b128(r, voff, soff, AUXG); }
__device__ __forceinline__ void st_sc(v4u v, rsrc_t r, unsigned voff, unsigned soff) { __builtin_amdgcn_raw_buffer_store_b128(v, r, voff, soff, AUX_SC1); }
__device__ __forceinline__ v4f mfma(v4u a, v4u b, v4f c) {
  return __builtin_amdgcn_mfma_f32_16x16x32_bf16(as_bf8(a), as_bf8(b), c, 0, 0, 0);
}
struct Tag { unsigned t32, t8; };
__device__ __forceinline__ v4u gran2(float a, float b, Tag t) {
  v4u g;
  g.x = ((as_u(a) + 0x80u) & 0xffffff00u) | t.t8; g.y = t.t32;
  g.z = ((as_u(b) + 0x80u) & 0xffffff00u) | t.t8; g.w = t.t32;
  return g;
}
__device__ __forceinline__ unsigned gbad(v4u g, Tag t) {
  unsigned x = g.x, y = g.y, z = g.z, w = g.w;
  return (((x ^ t.t8) | (z ^ t.t8)) & 0xffu) | (y ^ t.t32) | (w ^ t.t32);
}
__device__ __forceinline__ float gv0(v4u g) { unsigned x = g.x; return as_f(x & 0xffffff00u); }
__device__ __forceinline__ float gv1(v4u g) { unsigned z = g.z; return as_f(z & 0xffffff00u); }
#define SB() __builtin_amdgcn_sched_barrier(0)
#define CBAR() asm volatile("" ::: "memory")
__device__ __forceinline__ void wg_barrier() {
  asm volatile("s_waitcnt lgkmcnt(0)" ::: "memory");
  __builtin_amdgcn_s_barrier();
  asm volatile("" ::: "memory");
}
__device__ __forceinline__ float bsel(unsigned m, float a, float b) { return as_f((as_u(a) & m) | (as_u(b) & ~m)); }
__device__ __forceinline__ unsigned dup_lo(unsigned p) { return __builtin_amdgcn_perm(p, p, 0x01000100u); }
__device__ __forceinline__ unsigned dup_hi(unsigned p) { return __builtin_amdgcn_perm(p, p, 0x03020302u); }

struct __attribute__((aligned(16))) Lds {
  unsigned P[6][8][2 * NP];
  float ow[6][256];
  float ml[6][4];
  float fu[6][256];
  unsigned qs[2][64];
  unsigned tabo[2][4][8];
  unsigned tabh[2][4][12];
  float gs[2];
  v2f hx[448];
  unsigned qflag, fcnt, ocnt, hflag, pvflag, vflag[3];
};
__device__ __forceinline__ unsigned lds_ld(unsigned* p) {
  return __builtin_amdgcn_readfirstlane(__hip_atomic_load(p, __ATOMIC_RELAXED, __HIP_MEMORY_SCOPE_WORKGROUP));
}
__device__ __forceinline__ void lds_wait(unsigned* p, unsigned v) {
#pragma nounroll
  for (int spin = 0; spin < SPIN_MAX * 16; ++spin) {
    CBAR();
    if (lds_ld(p) >= v) break;
    __builtin_amdgcn_s_sleep(1);
  }
  CBAR();
}
__device__ __forceinline__ void lds_set(unsigned* p) {
  asm volatile("s_waitcnt lgkmcnt(0)" ::: "memory");
  __hip_atomic_store(p, 1u, __ATOMIC_RELAXED, __HIP_MEMORY_SCOPE_WORKGROUP);
  CBAR();
}
__device__ __forceinline__ void lds_inc(unsigned* p, int l) {
  asm volatile("s_waitcnt lgkmcnt(0)" ::: "memory");
  if (l == 0) __hip_atomic_fetch_add(p, 1u, __ATOMIC_RELAXED, __HIP_MEMORY_SCOPE_WORKGROUP);
  CBAR();
}
__device__ __forceinline__ void wait_until(u64 t) {
#pragma nounroll
  for (int spin = 0; spin < 4096; ++spin) {
    if (__builtin_amdgcn_s_memrealtime() >= t) break;
    __builtin_amdgcn_s_sleep(4);
  }
}
__device__ __forceinline__ float swap16_sum(float t) {
  auto a = __builtin_amdgcn_permlane16_swap(as_u(t), as_u(t), false, false);
  unsigned a0 = a[0], a1 = a[1];
  return as_f(a0) + as_f(a1);
}
__device__ __forceinline__ float swap16_max(float t) {
  auto a = __builtin_amdgcn_permlane16_swap(as_u(t), as_u(t), false, false);
  unsigned a0 = a[0], a1 = a[1];
  return fmaxf(as_f(a0), as_f(a1));
}
__device__ __forceinline__ float swap32_sum(float t) {
  auto a = __builtin_amdgcn_permlane32_swap(as_u(t), as_u(t), false, false);
  unsigned a0 = a[0], a1 = a[1];
  return as_f(a0) + as_f(a1);
}
__device__ __forceinline__ float swap32_max(float t) {
  auto a = __builtin_amdgcn_permlane32_swap(as_u(t), as_u(t), false, false);
  unsigned a0 = a[0], a1 = a[1];
  return fmaxf(as_f(a0), as_f(a1));
}

constexpr float SCL = 0.08838834764831845f * 1.4426950408889634f;
__device__ __forceinline__ void ld_opt(v4u (&r)[2 * NP], rsrc_t rs, unsigned lo, int np) {
#pragma unroll
  for (int tp = NPMIN; tp < NP; ++tp) {
    const int rp = tp - NPMIN;
    r[2 * rp] = v4u{0, 0, 0, 0}; r[2 * rp + 1] = v4u{0, 0, 0, 0};
    if (np > tp) { r[2 * rp] = ld_nt(rs, lo + (tp & 1) * 2048, (tp >> 1) * 4096); r[2 * rp + 1] = ld_nt(rs, lo + (tp & 1) * 2048 + 1024, (tp >> 1) * 4096); }
  }
  SB();
}
template <int T0, int T1>
__device__ __forceinline__ void ld_fix(v4u (&r)[2 * NP], rsrc_t rs, unsigned lo) {
#pragma unroll
  for (int tp = T0; tp < T1; ++tp) {
    const int rp = tp + NOPT;
    r[2 * rp] = ld_nt(rs, lo + (tp & 1) * 2048, (tp >> 1) * 4096);
    SB();
    r[2 * rp + 1] = ld_nt(rs, lo + (tp & 1) * 2048 + 1024, (tp >> 1) * 4096);
    SB();
  }
}
constexpr int vmcnt_imm(int n) { return (n & 15) | (7 << 4) | (15 << 8) | ((n >> 4) << 14); }

__global__ void __launch_bounds__(512, 2)
fused_decode(const u16* x_, const u16* kc_, const u16* vc_, const u16* wq_, const u16* wo_, const u16* wg_, const u16* wu_,
             const u16* wd_, const u16* gw_, u16* out_, unsigned char* ws_) {
  __shared__ Lds L;
  v16u ka; v4u kb; v2u kd;
  int bid = blockIdx.x;
  asm volatile("s_load_dwordx16 %0, %4, 0x0\n\ts_load_dwordx4 %1, %4, 0x40\n\ts_load_dwordx2 %2, %4, 0x50"
               : "=&s"(ka), "=&s"(kb), "=&s"(kd), "+s"(bid) : "s"(__builtin_amdgcn_kernarg_segment_ptr()));
  SB();
  const int tid = threadIdx.x, l = tid & 63;
  const int w = __builtin_amdgcn_readfirstlane(tid >> 6);
  bid = (bid >> 6 << 6) | (((bid + (bid >> 3)) & 7) << 3) | ((bid >> 3) & 7);
  const int xc = bid & 7, slot = bid >> 3, p4 = slot >> 3;
  const int kvh = 2 * xc + (slot >> 4), b = p4 & 1, s = slot & 7, j = slot & 15;
  const int pr = p4 * 8 + xc;
  const bool isT = s < 6;
  const bool isF = (s < 5) | ((s == 5) & (pr >= 3)) | ((s == 6) & (pr < 27));
  const unsigned tu = 6 * pr + s - min(pr, 3), tu7 = (tu * 9363u) >> 16;
  const int frest = s == 6 ? pr : (int)tu7, flo = s == 6 ? 0 : (int)(1 + tu - 7 * tu7);
  const int tj = b * 6 + s, rg = (tj * 43) >> 7, cg = tj - 3 * rg, R = kvh * 4 + rg;
  const int g = l >> 4, c = l & 15, e = l & 7, hb = (l >> 3) & 1;
  STAMP(0);
  const u64 tstart = __builtin_amdgcn_s_memrealtime();
  const int nT = pr < 3 ? NTA : (pr < 27 ? NTB : NTC), n5 = pr < 3 ? N5A : nT, n6 = pr < 3 ? N6A : (pr < 27 ? N6B : N6C);
  const int wg0 = min(s, 5) * nT + (s >= 6 ? n5 : 0) + (s == 7 ? n6 : 0);
  const int nq = s < 5 ? nT : (s == 5 ? n5 : (s == 6 ? n6 : 512 - wg0));
  const int wi = w < 4 ? w - 1 : w - 2;
  const int nb = (nq * 43) >> 8, nr = nq - 6 * nb;
  int np = nb + (wi < nr ? 1 : 0);
  unsigned kvq = (b * 16 + kvh) * 512 + wg0 + wi * nb + min(wi, nr);
  int fhi = (frest * 43) >> 7;
  int fks = frest - 3 * fhi;
  v4u dm;
  {
    const unsigned hm = (e & 1) ? 0xffff0000u : 0x0000ffffu;
    dm.x = (e >> 1) == 0 ? hm : 0u; dm.y = (e >> 1) == 1 ? hm : 0u; dm.z = (e >> 1) == 2 ? hm : 0u; dm.w = (e >> 1) == 3 ? hm : 0u;
  }
  int flo3 = flo + 3;
  asm volatile("" : "+s"(np), "+s"(kvq), "+s"(fhi), "+s"(flo3), "+s"(fks));
  SB();
  asm volatile("s_waitcnt lgkmcnt(0)" : "+s"(ka), "+s"(kb), "+s"(kd));
  SB();
  STAMP(7);
#define KPTR(lo, hi) (((u64)(hi) << 32) | (lo))
  const u16* x = (const u16*)KPTR(ka[0], ka[1]);
  const u16* kc = (const u16*)KPTR(ka[2], ka[3]);
  const u16* vc = (const u16*)KPTR(ka[4], ka[5]);
  const u16* wq = (const u16*)KPTR(ka[6], ka[7]);
  const u16* wo = (const u16*)KPTR(ka[8], ka[9]);
  const u16* wg = (const u16*)KPTR(ka[10], ka[11]);
  const u16* wu = (const u16*)KPTR(ka[12], ka[13]);
  const u16* wd = (const u16*)KPTR(ka[14], ka[15]);
  const u16* gw = (const u16*)KPTR(kb[0], kb[1]);
  u16* out = (u16*)KPTR(kb[2], kb[3]);
  unsigned char* ws = (unsigned char*)KPTR(kd[0], kd[1]);
  const int fcbg = 8 * fhi + ((flo3 - (int)((uintptr_t)wg >> 7)) & 7);
  const int fcbu = 8 * fhi + ((flo3 - (int)((uintptr_t)wu >> 7)) & 7);
  const rsrc_t rws = mk_rsrc(ws, WS_END), rx = mk_rsrc(x, 2 * DM * 2);
  const v4u srd = {kd[0], kd[1] & 0xffffu, WS_END, 0x00020000u};
  u64 ep;
  Tag tag;
#define LOAD_EPOCH() do { const unsigned char* p_ = ws; asm volatile("" : "+s"(p_) :: "memory"); ep = *(const __attribute__((address_space(4))) u64*)(uintptr_t)(p_ + WS_EPOCH); } while (0)
#define PIN_EPOCH() do { SB(); asm volatile("" : "+s"(ep) :: "memory"); ep += 1ull; tag.t32 = (unsigned)ep; tag.t8 = (unsigned)(ep >> 32) & 0xffu; } while (0)
  auto o_gather = [&](int bw) __attribute__((always_inline)) {
    const unsigned off = WS_O1 + (kvh * 16 + (l >> 2)) * 2048 + rg * 512 + bw * 128 + (l & 3) * 16;
    const unsigned offm = WS_ML + ((kvh * 2 + (rg >> 1)) * 16 + (l >> 2)) * 16;
    v4u A0, A1, AM, B0, B1, BM, C0, C1, CM;
    float a0x, a0z, a1x, a1z, amx, amz;
    unsigned e8, e32, u8, u32, cnt;
    u64 pm, sv;
    asm volatile(
        "s_mov_b64 %[sv], exec\n\ts_mov_b64 %[pm], exec\n\ts_mov_b32 %[cnt], %[lim]\n\t"
        "buffer_load_dwordx4 v[128:131], %[om], %[srd], 0 offen sc1\n\t"
        "buffer_load_dwordx4 v[132:135], %[o0], %[srd], 0 offen sc1\n\t"
        "buffer_load_dwordx4 v[136:139], %[o0], %[srd], 0 offen offset:64 sc1\n\t"
        "s_sleep %[gap]\n\t"
        "buffer_load_dwordx4 v[140:143], %[om], %[srd], 0 offen sc1\n\t"
        "buffer_load_dwordx4 v[144:147], %[o0], %[srd], 0 offen sc1\n\t"
        "buffer_load_dwordx4 v[148:151], %[o0], %[srd], 0 offen offset:64 sc1\n\t"
        "s_sleep %[gap]\n\t"
        "buffer_load_dwordx4 v[152:155], %[om], %[srd], 0 offen sc1\n\t"
        "buffer_load_dwordx4 v[156:159], %[o0], %[srd], 0 offen sc1\n\t"
        "buffer_load_dwordx4 v[160:163], %[o0], %[srd], 0 offen offset:64 sc1\n\t"
        ".Log_loop%=:\n\t"
        "s_waitcnt vmcnt(8)\n\t"
        "v_bitop3_b32 %[e8], v128, v130, %[t8] bitop3:0x7e\n\t"
        "v_bitop3_b32 %[e32], v129, v131, %[t32] bitop3:0x7e\n\t"
        "s_waitcnt vmcnt(7)\n\t"
        "v_bitop3_b32 %[u8], v132, v134, %[t8] bitop3:0x7e\n\t"
        "v_bitop3_b32 %[u32], v133, v135, %[t32] bitop3:0x7e\n\t"
        "v_or_b32 %[e8], %[e8], %[u8]\n\t"
        "v_or_b32 %[e32], %[e32], %[u32]\n\t"
        "s_waitcnt vmcnt(6)\n\t"
        "v_bitop3_b32 %[u8], v136, v138, %[t8] bitop3:0x7e\n\t"
        "v_bitop3_b32 %[u32], v137, v139, %[t32] bitop3:0x7e\n\t"
        "v_bitop3_b32 %[e8], %[e8], %[u8], %[ff] bitop3:0xa8\n\t"
        "v_or3_b32 %[e8], %[e8], %[e32], %[u32]\n\t"
        "v_cmp_eq_u32 vcc, 0, %[e8]\n\t"
        "s_and_b64 exec, vcc, %[pm]\n\t"
        "v_and_b32 %[amx], %[msk], v128\n\t"
        "v_and_b32 %[amz], %[msk], v130\n\t"
        "v_and_b32 %[a0x], %[msk], v132\n\t"
        "v_and_b32 %[a0z], %[msk], v134\n\t"
        "v_and_b32 %[a1x], %[msk], v136\n\t"
        "v_and_b32 %[a1z], %[msk], v138\n\t"
        "s_andn2_b64 %[pm], %[pm], exec\n\t"
        "s_cbranch_scc0 .Log_done%=\n\t"
        "s_mov_b64 exec, %[pm]\n\t"
        "buffer_load_dwordx4 v[128:131], %[om], %[srd], 0 offen sc1\n\t"
        "buffer_load_dwordx4 v[132:135], %[o0], %[srd], 0 offen sc1\n\t"
        "buffer_load_dwordx4 v[136:139], %[o0], %[srd], 0 offen offset:64 sc1\n\t"
        "s_mov_b64 exec, %[sv]\n\t"
        "s_waitcnt vmcnt(8)\n\t"
        "v_bitop3_b32 %[e8], v140, v142, %[t8] bitop3:0x7e\n\t"
        "v_bitop3_b32 %[e32], v141, v143, %[t32] bitop3:0x7e\n\t"
        "s_waitcnt vmcnt(7)\n\t"
        "v_bitop3_b32 %[u8], v144, v146, %[t8] bitop3:0x7e\n\t"
        "v_bitop3_b32 %[u32], v145, v147, %[t32] bitop3:0x7e\n\t"
        "v_or_b32 %[e8], %[e8], %[u8]\n\t"
        "v_or_b32 %[e32], %[e32], %[u32]\n\t"
        "s_waitcnt vmcnt(6)\n\t"
        "v_bitop3_b32 %[u8], v148, v150, %[t8] bitop3:0x7e\n\t"
        "v_bitop3_b32 %[u32], v149, v151, %[t32] bitop3:0x7e\n\t"
        "v_bitop3_b32 %[e8], %[e8], %[u8], %[ff] bitop3:0xa8\n\t"
        "v_or3_b32 %[e8], %[e8], %[e32], %[u32]\n\t"
        "v_cmp_eq_u32 vcc, 0, %[e8]\n\t"
        "s_and_b64 exec, vcc, %[pm]\n\t"
        "v_and_b32 %[amx], %[msk], v140\n\t"
        "v_and_b32 %[amz], %[msk], v142\n\t"
        "v_and_b32 %[a0x], %[msk], v144\n\t"
        "v_and_b32 %[a0z], %[msk], v146\n\t"
        "v_and_b32 %[a1x], %[msk], v148\n\t"
        "v_and_b32 %[a1z], %[msk], v150\n\t"
        "s_andn2_b64 %[pm], %[pm], exec\n\t"
        "s_cbranch_scc0 .Log_done%=\n\t"
        "s_mov_b64 exec, %[pm]\n\t"
        "buffer_load_dwordx4 v[140:143], %[om], %[srd], 0 offen sc1\n\t"
        "buffer_load_dwordx4 v[144:147], %[o0], %[srd], 0 offen sc1\n\t"
        "buffer_load_dwordx4 v[148:151], %[o0], %[srd], 0 offen offset:64 sc1\n\t"
        "s_mov_b64 exec, %[sv]\n\t"
        "s_waitcnt vmcnt(8)\n\t"
        "v_bitop3_b32 %[e8], v152, v154, %[t8] bitop3:0x7e\n\t"
        "v_bitop3_b32 %[e32], v153, v155, %[t32] bitop3:0x7e\n\t"
        "s_waitcnt vmcnt(7)\n\t"
        "v_bitop3_b32 %[u8], v156, v158, %[t8] bitop3:0x7e\n\t"
        "v_bitop3_b32 %[u32], v157, v159, %[t32] bitop3:0x7e\n\t"
        "v_or_b32 %[e8], %[e8], %[u8]\n\t"
        "v_or_b32 %[e32], %[e32], %[u32]\n\t"
        "s_waitcnt vmcnt(6)\n\t"
        "v_bitop3_b32 %[u8], v160, v162, %[t8] bitop3:0x7e\n\t"
        "v_bitop3_b32 %[u32], v161, v163, %[t32] bitop3:0x7e\n\t"
        "v_bitop3_b32 %[e8], %[e8], %[u8], %[ff] bitop3:0xa8\n\t"
        "v_or3_b32 %[e8], %[e8], %[e32], %[u32]\n\t"
        "v_cmp_eq_u32 vcc, 0, %[e8]\n\t"
        "s_and_b64 exec, vcc, %[pm]\n\t"
        "v_and_b32 %[amx], %[msk], v152\n\t"
        "v_and_b32 %[amz], %[msk], v154\n\t"
        "v_and_b32 %[a0x], %[msk], v156\n\t"
        "v_and_b32 %[a0z], %[msk], v158\n\t"
        "v_and_b32 %[a1x], %[msk], v160\n\t"
        "v_and_b32 %[a1z], %[msk], v162\n\t"
        "s_andn2_b64 %[pm], %[pm], exec\n\t"
        "s_cbranch_scc0 .Log_done%=\n\t"
        "s_mov_b64 exec, %[pm]\n\t"
        "buffer_load_dwordx4 v[152:155], %[om], %[srd], 0 offen sc1\n\t"
        "buffer_load_dwordx4 v[156:159], %[o0], %[srd], 0 offen sc1\n\t"
        "buffer_load_dwordx4 v[160:163], %[o0], %[srd], 0 offen offset:64 sc1\n\t"
        "s_mov_b64 exec, %[sv]\n\t"
        "s_sub_u32 %[cnt], %[cnt], 1\n\ts_cbranch_scc0 .Log_loop%=\n\t"
        ".Log_done%=:\n\ts_mov_b64 exec, %[sv]\n\ts_nop 1"
        : "=&{v[128:131]}"(AM), "=&{v[132:135]}"(A0), "=&{v[136:139]}"(A1), "=&{v[140:143]}"(BM), "=&{v[144:147]}"(B0), "=&{v[148:151]}"(B1),
          "=&{v[152:155]}"(CM), "=&{v[156:159]}"(C0), "=&{v[160:163]}"(C1),
          [a0x] "=&v"(a0x), [a0z] "=&v"(a0z), [a1x] "=&v"(a1x), [a1z] "=&v"(a1z), [amx] "=&v"(amx), [amz] "=&v"(amz),
          [e8] "=&v"(e8), [e32] "=&v"(e32), [u8] "=&v"(u8), [u32] "=&v"(u32), [pm] "=&s"(pm), [sv] "=&s"(sv), [cnt] "=&s"(cnt)
        : [o0] "v"(off), [om] "v"(offm), [srd] "s"(srd), [t32] "s"(tag.t32), [t8] "s"(tag.t8), [ff] "s"(0xffu), [msk] "s"(0xffffff00u),
          [lim] "i"(SPIN_MAX), [gap] "i"(PGAP)
        : "vcc", "scc", "memory");
    lds_wait(&L.hflag, 1);
    const float gsb = L.gs[l >> 5];
    const float m = amx;
    float M = fmaxf(m, dpp<DPP_ROR4>(m)); M = fmaxf(M, dpp<DPP_ROR8>(M)); M = swap16_max(M);
    const float fe = __builtin_amdgcn_exp2f(m - M);
    float Ls = fe * amz;
    Ls += dpp<DPP_ROR4>(Ls); Ls += dpp<DPP_ROR8>(Ls); Ls = swap16_sum(Ls);
    float v0 = fe * a0x, v1 = fe * a0z, v2 = fe * a1x, v3 = fe * a1z;
    v0 += dpp<DPP_ROR4>(v0); v1 += dpp<DPP_ROR4>(v1); v2 += dpp<DPP_ROR4>(v2); v3 += dpp<DPP_ROR4>(v3);
    v0 += dpp<DPP_ROR8>(v0); v1 += dpp<DPP_ROR8>(v1); v2 += dpp<DPP_ROR8>(v2); v3 += dpp<DPP_ROR8>(v3);
    v0 = swap16_sum(v0); v1 = swap16_sum(v1); v2 = swap16_sum(v2); v3 = swap16_sum(v3);
    const float fct = gsb * __builtin_amdgcn_rcpf(Ls);
    if ((l & 28) == 0) { v2u t = {pk_bf16(v0 * fct, v1 * fct), pk_bf16(v2 * fct, v3 * fct)}; *(v2u*)&L.tabo[l >> 5][l & 3][2 * bw] = t; }
    STAMP(10);
    wg_barrier();
    asm volatile("s_waitcnt vmcnt(0)" :: "{v[128:131]}"(AM), "{v[132:135]}"(A0), "{v[136:139]}"(A1), "{v[140:143]}"(BM), "{v[144:147]}"(B0), "{v[148:151]}"(B1),
                 "{v[152:155]}"(CM), "{v[156:159]}"(C0), "{v[160:163]}"(C1));
  };
  if (w == 0) {
    __builtin_amdgcn_s_setprio(3);
    const int qcb = 8 * xc + ((s + 4 - (int)((uintptr_t)wq >> 7)) & 7), kq = p4;
    unsigned xa[2][9];
    v4u wr[18], gx[3], gg[3];
    {
      const rsrc_t rq = mk_rsrc(wq + (size_t)kq * 144 * 4096 + qcb * 64, 144 * 8192);
      const unsigned vo = (72 * hb + 18 * g) * 8192 + e * 16;
#pragma unroll
      for (int r = 0; r < 2; ++r) {
        const unsigned xo = (hb * DM + kq * 144 + 72 * r + 18 * g) * 2;
        v4u t0 = __builtin_amdgcn_raw_buffer_load_b128(rx, xo, 0, 0), t1 = __builtin_amdgcn_raw_buffer_load_b128(rx, xo + 16, 0, 0);
        xa[r][0] = t0.x; xa[r][1] = t0.y; xa[r][2] = t0.z; xa[r][3] = t0.w;
        xa[r][4] = t1.x; xa[r][5] = t1.y; xa[r][6] = t1.z; xa[r][7] = t1.w;
        xa[r][8] = __builtin_amdgcn_raw_buffer_load_b32(rx, xo + 32, 0, 0);
      }
      SB();
#pragma unroll
      for (int i = 0; i < 18; ++i) { wr[i] = ld_nt(rq, vo, i * 8192); SB(); }
    }
    __builtin_amdgcn_s_barrier();
    LOAD_EPOCH();
    if (s == 6) {
      const rsrc_t rg_ = mk_rsrc(gw, DM * 2);
#pragma unroll
      for (int i = 0; i < 3; ++i) {
        const int ck = (l & 31) + 32 * i;
        gg[i] = __builtin_amdgcn_raw_buffer_load_b128(rg_, ck * 16, 0, 0);
        gx[i] = __builtin_amdgcn_raw_buffer_load_b128(rx, ((l >> 5) * 72 + ck) * 16, 0, 0);
      }
    }
    SB();
    PIN_EPOCH();
    {
      v4f d0 = {0, 0, 0, 0}, d1 = {0, 0, 0, 0};
#pragma unroll
      for (int i = 0; i < 18; ++i) {
        const unsigned p0 = (i & 1) ? dup_hi(xa[0][i >> 1]) : dup_lo(xa[0][i >> 1]);
        const unsigned p1 = (i & 1) ? dup_hi(xa[1][i >> 1]) : dup_lo(xa[1][i >> 1]);
        d0 = mfma(dm & p0, wr[i], d0);
        d1 = mfma(dm & p1, wr[i], d1);
      }
      float r0 = hb ? d1.x : d0.x, r1 = hb ? d1.y : d0.y, r2 = hb ? d1.z : d0.z, r3 = hb ? d1.w : d0.w;
      r0 += dpp<DPP_ROR8>(r0); r1 += dpp<DPP_ROR8>(r1); r2 += dpp<DPP_ROR8>(r2); r3 += dpp<DPP_ROR8>(r3);
      st_sc(gran2(hb ? r2 : r0, hb ? r3 : r1, tag), rws, WS_QB + (((qcb >> 2) * 2 + (l >> 5)) * 4 + kq) * 2048 + (qcb & 3) * 512 + (l & 31) * 16, 0);
      SB();
    }
    STAMP(1);
    if (s == 6) {
      float gs = 0.f;
#pragma unroll
      for (int i = 0; i < 3; ++i) {
        const unsigned a0 = gg[i].x, a1 = gg[i].y, a2 = gg[i].z, a3 = gg[i].w, b0 = gx[i].x, b1 = gx[i].y, b2 = gx[i].z, b3 = gx[i].w;
        gs = __builtin_amdgcn_fdot2_f32_bf16(as_bf2(a0), as_bf2(b0), gs, false);
        gs = __builtin_amdgcn_fdot2_f32_bf16(as_bf2(a1), as_bf2(b1), gs, false);
        gs = __builtin_amdgcn_fdot2_f32_bf16(as_bf2(a2), as_bf2(b2), gs, false);
        gs = __builtin_amdgcn_fdot2_f32_bf16(as_bf2(a3), as_bf2(b3), gs, false);
      }
      gs = sum8(gs); gs += dpp<DPP_ROR8>(gs); gs = swap16_sum(gs);
      gs = 1.f / (1.f + __expf(-gs));
      st_sc(gran2(gs, as_f(__builtin_amdgcn_readlane(as_u(gs), 32)), tag), rws, l == 0 ? WS_G + pr * 64 : OOR, 0);
    }
    SB();
    if (isF) {
      lds_wait(&L.fcnt, 6);
      STAMP(2);
      const int ch = l >> 3;
      float ga[2] = {0.f, 0.f}, up[2] = {0.f, 0.f};
#pragma unroll
      for (int bb = 0; bb < 2; ++bb)
#pragma unroll
        for (int ww = 0; ww < 6; ++ww) { ga[bb] += L.fu[ww][ch * 16 + bb * 8 + e]; up[bb] += L.fu[ww][(8 + ch) * 16 + bb * 8 + e]; }
      const unsigned off = WS_HB + (fks * 2 * FF + l) * 16;
      st_sc(gran2(ga[0], ga[1], tag), rws, off, fcbg * 1024);
      st_sc(gran2(up[0], up[1], tag), rws, off, FF * 16 + fcbu * 1024);
      SB();
      STAMP(3);
    }
    lds_wait(&L.ocnt, 6);
    STAMP(4);
    {
      float m0 = -INFINITY, m1 = -INFINITY;
      v4f mm[6];
#pragma unroll
      for (int ww = 0; ww < 6; ++ww) { mm[ww] = *(const v4f*)&L.ml[ww][0]; m0 = fmaxf(m0, mm[ww].x); m1 = fmaxf(m1, mm[ww].z); }
      v2f t0 = {0.f, 0.f}, t1 = {0.f, 0.f};
      float l0 = 0.f, l1 = 0.f;
#pragma unroll
      for (int ww = 0; ww < 6; ++ww) {
        const float f0 = __builtin_amdgcn_exp2f(mm[ww].x - m0), f1 = __builtin_amdgcn_exp2f(mm[ww].z - m1);
        t0 += f0 * *(const v2f*)&L.ow[ww][2 * l]; t1 += f1 * *(const v2f*)&L.ow[ww][128 + 2 * l];
        l0 += f0 * mm[ww].y; l1 += f1 * mm[ww].w;
      }
      const unsigned off = WS_O1 + (kvh * 16 + j) * 2048 + l * 16;
      st_sc(gran2(t0.x, t0.y, tag), rws, off, 0);
      st_sc(gran2(t1.x, t1.y, tag), rws, off + 1024, 0);
      v4u gm = gran2(0.f, l ? l1 : l0, tag);
      gm.x = as_u(l ? m1 : m0) | tag.t8;
      st_sc(gm, rws, l < 2 ? WS_ML + ((kvh * 2 + l) * 16 + j) * 16 : OOR, 0);
      lds_set(&L.pvflag);
      STAMP(5);
    }
    if (isT) wg_barrier();
  } else if (w == 4) {
    __builtin_amdgcn_s_setprio(3);
    if (l < 8) (&L.qflag)[l] = 0;
    wg_barrier();
    LOAD_EPOCH();
    PIN_EPOCH();
    {
      const unsigned qoff = WS_QB + (kvh * 2 + b) * 8192 + l * 16;
      unsigned qoff2 = qoff + 4096;
      asm volatile("" : "+v"(qoff2));
      v4u qa[8];
      wait_until(tstart + TQ);
      STAMP(3);
#pragma unroll
      for (int i = 0; i < 8; ++i) qa[i] = ld_g(rws, (i < 4 ? qoff : qoff2) + (i & 3) * 1024, 0);
      SB();
      STAMP(11);
      unsigned pend = 0xff;
#pragma nounroll
      for (int spin = 0; spin < SPIN_MAX; ++spin) {
#pragma unroll
        for (int i = 0; i < 8; ++i) if (__builtin_amdgcn_ballot_w64(gbad(qa[i], tag) != 0) == 0) pend &= ~(1u << i);
        if (pend == 0) break;
        __builtin_amdgcn_s_sleep(2);
        CBAR();
#pragma unroll
        for (int i = 0; i < 8; ++i) if (pend >> i & 1) qa[i] = ld_g(rws, qoff, i * 1024);
      }
      const int pi = (l >> 5) * 32 + 4 * e + 2 * (g & 1) + hb;
#pragma unroll
      for (int h = 0; h < 2; ++h) {
        const float a0 = (gv0(qa[h]) + gv0(qa[2 + h])) + (gv0(qa[4 + h]) + gv0(qa[6 + h]));
        const float a1 = (gv1(qa[h]) + gv1(qa[2 + h])) + (gv1(qa[4 + h]) + gv1(qa[6 + h]));
        L.qs[h][pi] = pk_bf16(a0, a1);
      }
      lds_set(&L.qflag);
    }
    STAMP(2);
    if (isT) {
      unsigned ho[7];
#pragma unroll
      for (int i = 0; i < 7; ++i) {
        const unsigned gi = 64 * i + l, km = (gi * 911u) >> 16;
        ho[i] = WS_HB + (km * (FF - 72) + R * 72 + gi) * 16;
      }
      ho[6] = l < 48 ? ho[6] : (l == 48 ? WS_G + pr * 64 : OOR);
      v4u a[7];
      wait_until(tstart + TH);
      STAMP(4);
#pragma unroll
      for (int i = 0; i < 7; ++i) a[i] = ld_g(rws, ho[i], 0);
      SB();
      unsigned pend = 0x7f;
#pragma nounroll
      for (int spin = 0; spin < SPIN_MAX; ++spin) {
#pragma unroll
        for (int i = 0; i < 7; ++i)
          if (__builtin_amdgcn_ballot_w64(gbad(a[i], tag) != 0 && (i < 6 || l <= 48)) == 0) pend &= ~(1u << i);
        if (pend == 0) break;
        __builtin_amdgcn_s_sleep(2);
        CBAR();
#pragma unroll
        for (int i = 0; i < 7; ++i) if (pend >> i & 1) a[i] = ld_g(rws, ho[i], 0);
      }
#pragma unroll
      for (int i = 0; i < 7; ++i) { v2f t = {gv0(a[i]), gv1(a[i])}; L.hx[64 * i + l] = t; }
      CBAR();
      const v2f gt = L.hx[432];
      if (l < 2) L.gs[l] = l ? gt.y : gt.x;
#pragma unroll
      for (int r = 0; r < 2; ++r) {
        const int rho = r ? 64 + e : l;
        v2f p[6];
#pragma unroll
        for (int i = 0; i < 6; ++i) p[i] = L.hx[72 * i + rho];
#pragma unroll
        for (int bb = 0; bb < 2; ++bb) {
          const float ga = rnd_bf16((p[0][bb] + p[2][bb]) + p[4][bb]);
          const float up = rnd_bf16((p[1][bb] + p[3][bb]) + p[5][bb]);
          const float si = rnd_bf16(ga / (1.f + __expf(-ga)));
          const float hv = rnd_bf16(si * up) * (1.f - gt[bb]);
          ((u16*)&L.tabh[bb][(rho >> 1) & 3][rho >> 3])[rho & 1] = (u16)pk_bf16(hv, hv);
        }
      }
      lds_set(&L.hflag);
      STAMP(5);
      lds_wait(&L.pvflag, 1);
      o_gather(0);
    }
  } else {
    __builtin_amdgcn_s_setprio(3);
    const size_t kvoff = (size_t)kvq * 1024;
    const rsrc_t rk = mk_rsrc(kc + kvoff, np * 2048), rv = mk_rsrc(vc + kvoff, np * 2048);
    const unsigned ko = ((l >> 2) & 3) * 256 + g * 64 + (l & 3) * 16;
    wg_barrier();
    STAMP(8);
    v4u kr[2 * NP], vr[2 * NP];
    {
      v4u fw[8], xf;
      if (isF) {
        xf = __builtin_amdgcn_raw_buffer_load_b128(rx, (hb * DM + fks * 192 + 32 * wi + 8 * g) * 2, 0, 0);
        const u16* fp = (hb ? wu + fcbu * 64 : wg + fcbg * 64) + (size_t)(fks * 192 + 32 * wi + 8 * g) * FF + e * 8;
#pragma unroll
        for (int i = 0; i < 8; ++i) fw[i] = __builtin_nontemporal_load((const GAS v4u*)(uintptr_t)(fp + (size_t)i * FF));
      }
      SB();
      ld_fix<0, KF>(kr, rk, ko);
      LOAD_EPOCH();
      if (w < 4) __builtin_amdgcn_s_setprio(2); else __builtin_amdgcn_s_setprio(1);
      PIN_EPOCH();
      __builtin_amdgcn_s_waitcnt(vmcnt_imm(2 * KF));
      SB();
      if (isF) {
        v4f d = {0, 0, 0, 0};
        const unsigned xx[4] = {xf.x, xf.y, xf.z, xf.w};
#pragma unroll
        for (int i = 0; i < 4; ++i) { d = mfma(dm & dup_lo(xx[i]), fw[2 * i], d); d = mfma(dm & dup_hi(xx[i]), fw[2 * i + 1], d); }
        *(v4f*)&L.fu[wi][c * 16 + 4 * g] = d;
        lds_inc(&L.fcnt, l);
        STAMP(10);
      }
      SB();
    }
    ld_fix<KF, NPMIN>(kr, rk, ko);
    ld_opt(kr, rk, ko, np);
    STAMP(1);
    asm volatile("" : "+s"(np));
    ld_opt(vr, rv, l * 16, np);
    ld_fix<0, NPMIN>(vr, rv, l * 16);
    __builtin_amdgcn_s_setprio(3);
    if (w > 4) lds_set(&L.vflag[w - 5]);
    SB();
    STAMP(2);
    lds_wait(&L.qflag, 1);
    STAMP(3);
    asm volatile("" : "+s"(np));
    {
      const v4u qv = *(const v4u*)&L.qs[(l >> 2) & 1][16 * g + 4 * (l & 3)];
      const v4u zz = {0, 0, 0, 0};
      const v4u qlo = hb ? zz : qv, qhi = hb ? qv : zz;
      unsigned m0 = (l & 1) ? ~0u : 0u, m1 = (l & 2) ? ~0u : 0u;
      asm volatile("" : "+v"(m0), "+v"(m1));
      float sel[NP / 4];
#pragma unroll
      for (int G0 = 1; G0 <= NP / 4; ++G0) {
        const int G = G0 % (NP / 4);
        float d[4][4];
#pragma unroll
        for (int p = 0; p < 4; ++p) {
          v4f t = {0, 0, 0, 0};
          t = mfma(qlo, kr[8 * G + 2 * p], t);
          t = mfma(qhi, kr[8 * G + 2 * p + 1], t);
          d[p][0] = t.x; d[p][1] = t.y; d[p][2] = t.z; d[p][3] = t.w;
        }
        float xx[2];
#pragma unroll
        for (int h2 = 0; h2 < 2; ++h2) {
          const float* A = d[2 * h2]; const float* B = d[2 * h2 + 1];
          const float keep = bsel(m1, bsel(m0, B[3], A[2]), bsel(m0, B[1], A[0]));
          const float send = bsel(m1, bsel(m0, A[3], B[2]), bsel(m0, A[1], B[0]));
          xx[h2] = keep + dpp<DPP_X1>(send);
        }
        const float sv = bsel(m1, xx[1], xx[0]) + dpp<DPP_X2>(bsel(m1, xx[0], xx[1]));
        const int rp = 4 * G + (l & 3);
        sel[G] = (4 * G >= NOPT || rp >= NOPT || rp + NPMIN < np) ? sv * SCL : -INFINITY;
      }
      float mx = sel[0];
#pragma unroll
      for (int G = 1; G < NP / 4; ++G) mx = fmaxf(mx, sel[G]);
      mx = fmaxf(mx, dpp<DPP_X1>(mx)); mx = fmaxf(mx, dpp<DPP_X2>(mx)); mx = fmaxf(mx, dpp<DPP_ROR4>(mx)); mx = fmaxf(mx, dpp<DPP_ROR8>(mx));
      mx = swap32_max(mx);
      mx = as_f(as_u(mx) & 0xffffff00u);
      float ls = 0.f;
#pragma unroll
      for (int G = 0; G < NP / 4; ++G) {
        const float p = __builtin_amdgcn_exp2f(sel[G] - mx);
        ls += p;
        L.P[wi][(l >> 2) & 7][8 * G + 2 * (l & 3) + (l >> 5)] = pk_bf16(p, p);
      }
      ls += dpp<DPP_X1>(ls); ls += dpp<DPP_X2>(ls); ls += dpp<DPP_ROR4>(ls); ls += dpp<DPP_ROR8>(ls);
      ls = swap32_sum(ls);
      if ((l & 47) == 0) { v2f t = {mx, ls}; *(v2f*)&L.ml[wi][2 * g] = t; }
    }
    SB();
    STAMP(6);
    {
      v4f o0 = {0, 0, 0, 0}, o1 = {0, 0, 0, 0};
#pragma unroll
      for (int k = 0; k < NP / 2; ++k) {
        const v4u pv = *(const v4u*)&L.P[wi][hb * 4 + g][4 * k];
        const unsigned p0 = pv.x, p1 = pv.y, p2 = pv.z, p3 = pv.w;
        o0 = mfma(dm & p0, vr[4 * k], o0); o1 = mfma(dm & p1, vr[4 * k + 1], o1);
        o0 = mfma(dm & p2, vr[4 * k + 2], o0); o1 = mfma(dm & p3, vr[4 * k + 3], o1);
      }
      *(v4f*)&L.ow[wi][(l >> 5) * 128 + 8 * c + 4 * (g & 1)] = o0 + o1;
    }
    lds_inc(&L.ocnt, l);
    STAMP(4);
    if (w < 4) {
      if (isT) {
        const int cbk = 3 * cg + w - 1;
        const rsrc_t ro = mk_rsrc(wo + (size_t)R * 64 * DM, 64 * DM * 2);
        const rsrc_t rd = mk_rsrc(wd + (size_t)R * 72 * DM, 72 * DM * 2);
        const unsigned to = (2 * g + hb) * 1152 + cbk * 128 + e * 16;
        lds_wait(&L.vflag[w - 1], 1);
        __builtin_amdgcn_s_setprio(0);
        v4u tw[17];
#pragma unroll
        for (int i = 0; i < 9; ++i) tw[i] = ld_nt(rd, to, i * 9216);
#pragma unroll
        for (int i = 0; i < 8; ++i) tw[9 + i] = ld_nt(ro, to, i * 9216);
        SB();
        __builtin_amdgcn_s_setprio(3);
        STAMP(5);
        lds_wait(&L.hflag, 1);
        v4f d0 = {0, 0, 0, 0}, d1 = {0, 0, 0, 0};
        {
          const v4u h0 = *(const v4u*)&L.tabh[hb][g][0], h1 = *(const v4u*)&L.tabh[hb][g][4];
          const unsigned hp[9] = {h0.x, h0.y, h0.z, h0.w, h1.x, h1.y, h1.z, h1.w, L.tabh[hb][g][8]};
#pragma unroll
          for (int i = 0; i < 9; ++i) { d0 = mfma(dm & dup_lo(hp[i]), tw[i], d0); d1 = mfma(dm & dup_hi(hp[i]), tw[i], d1); }
        }
        asm volatile("" :: "v"(tw[16]));
        SB();
        STAMP(11);
        wg_barrier();
        STAMP(7);
        {
          const v4u o0 = *(const v4u*)&L.tabo[hb][g][0], o1 = *(const v4u*)&L.tabo[hb][g][4];
          const unsigned op[8] = {o0.x, o0.y, o0.z, o0.w, o1.x, o1.y, o1.z, o1.w};
#pragma unroll
          for (int i = 0; i < 8; ++i) { d0 = mfma(dm & dup_lo(op[i]), tw[9 + i], d0); d1 = mfma(dm & dup_hi(op[i]), tw[9 + i], d1); }
        }
        float r0 = hb ? d1.x : d0.x, r1 = hb ? d1.y : d0.y, r2 = hb ? d1.z : d0.z, r3 = hb ? d1.w : d0.w;
        r0 += dpp<DPP_ROR8>(r0); r1 += dpp<DPP_ROR8>(r1); r2 += dpp<DPP_ROR8>(r2); r3 += dpp<DPP_ROR8>(r3);
        st_sc(gran2(hb ? r2 : r0, hb ? r3 : r1, tag), rws, WS_FB + (cbk * 64 + R) * 1024 + l * 16, 0);
        STAMP(8);
      } else {
        const int nt = (kvh * 2 + b) * 2 + (s & 1);
        const int rw = w < 3 ? nt * 2 + (w - 1) : 128 + nt;
        if (rw < 144) {
          __builtin_amdgcn_s_setprio(0);
          lds_wait(&L.pvflag, 1);
          STAMP(5);
          const unsigned off = WS_FB + (rw >> 4) * 65536 + (l >> 2) * 4096 + (rw & 15) * 64 + (l & 3) * 16;
#pragma nounroll
          for (int spin = 0; spin < SPIN_MAX; ++spin) {
            CBAR();
            const v4u a = ld_sc(rws, off, 0);
            if (__builtin_amdgcn_ballot_w64(gbad(a, tag) == 0) != 0) break;
            __builtin_amdgcn_s_sleep(4);
          }
          v4u A0, A1, A2, A3, B0, B1, B2, B3, C0, C1, C2, C3;
          float v0, v1, p0, p1, q0, q1;
          unsigned e8, e32, u8, u32, cnt;
          u64 pm, sv;
          asm volatile(
              "s_mov_b64 %[sv], exec\n\ts_mov_b64 %[pm], exec\n\ts_mov_b32 %[cnt], %[lim]\n\t"
              "buffer_load_dwordx4 v[128:131], %[o0], %[srd], 0 offen offset:0 sc1\n\t"
              "buffer_load_dwordx4 v[132:135], %[o0], %[srd], 0 offen offset:1024 sc1\n\t"
              "buffer_load_dwordx4 v[136:139], %[o0], %[srd], 0 offen offset:2048 sc1\n\t"
              "buffer_load_dwordx4 v[140:143], %[o0], %[srd], 0 offen offset:3072 sc1\n\t"
              "s_sleep %[gap]\n\t"
              "buffer_load_dwordx4 v[144:147], %[o0], %[srd], 0 offen offset:0 sc1\n\t"
              "buffer_load_dwordx4 v[148:151], %[o0], %[srd], 0 offen offset:1024 sc1\n\t"
              "buffer_load_dwordx4 v[152:155], %[o0], %[srd], 0 offen offset:2048 sc1\n\t"
              "buffer_load_dwordx4 v[156:159], %[o0], %[srd], 0 offen offset:3072 sc1\n\t"
              "s_sleep %[gap]\n\t"
              "buffer_load_dwordx4 v[160:163], %[o0], %[srd], 0 offen offset:0 sc1\n\t"
              "buffer_load_dwordx4 v[164:167], %[o0], %[srd], 0 offen offset:1024 sc1\n\t"
              "buffer_load_dwordx4 v[168:171], %[o0], %[srd], 0 offen offset:2048 sc1\n\t"
              "buffer_load_dwordx4 v[172:175], %[o0], %[srd], 0 offen offset:3072 sc1\n\t"
              ".Lrd_loop%=:\n\t"
              "s_waitcnt vmcnt(11)\n\t"
              "v_bitop3_b32 %[e8], v128, v130, %[t8] bitop3:0x7e\n\t"
              "v_bitop3_b32 %[e32], v129, v131, %[t32] bitop3:0x7e\n\t"
              "v_and_b32 %[p0], %[msk], v128\n\t"
              "v_and_b32 %[p1], %[msk], v130\n\t"
              "s_waitcnt vmcnt(10)\n\t"
              "v_bitop3_b32 %[u8], v132, v134, %[t8] bitop3:0x7e\n\t"
              "v_bitop3_b32 %[u32], v133, v135, %[t32] bitop3:0x7e\n\t"
              "v_and_b32 %[q0], %[msk], v132\n\t"
              "v_and_b32 %[q1], %[msk], v134\n\t"
              "v_or_b32 %[e8], %[e8], %[u8]\n\t"
              "v_or_b32 %[e32], %[e32], %[u32]\n\t"
              "v_add_f32 %[p0], %[p0], %[q0]\n\t"
              "v_add_f32 %[p1], %[p1], %[q1]\n\t"
              "s_waitcnt vmcnt(9)\n\t"
              "v_bitop3_b32 %[u8], v136, v138, %[t8] bitop3:0x7e\n\t"
              "v_bitop3_b32 %[u32], v137, v139, %[t32] bitop3:0x7e\n\t"
              "v_and_b32 %[q0], %[msk], v136\n\t"
              "v_and_b32 %[q1], %[msk], v138\n\t"
              "v_or_b32 %[e8], %[e8], %[u8]\n\t"
              "v_or_b32 %[e32], %[e32], %[u32]\n\t"
              "v_add_f32 %[p0], %[p0], %[q0]\n\t"
              "v_add_f32 %[p1], %[p1], %[q1]\n\t"
              "s_waitcnt vmcnt(8)\n\t"
              "v_bitop3_b32 %[u8], v140, v142, %[t8] bitop3:0x7e\n\t"
              "v_bitop3_b32 %[u32], v141, v143, %[t32] bitop3:0x7e\n\t"
              "v_bitop3_b32 %[e8], %[e8], %[u8], %[ff] bitop3:0xa8\n\t"
              "v_or3_b32 %[e8], %[e8], %[e32], %[u32]\n\t"
              "v_cmp_eq_u32 vcc, 0, %[e8]\n\t"
              "v_and_b32 %[q0], %[msk], v140\n\t"
              "v_and_b32 %[q1], %[msk], v142\n\t"
              "s_and_b64 exec, vcc, %[pm]\n\t"
              "v_add_f32 %[v0], %[p0], %[q0]\n\t"
              "v_add_f32 %[v1], %[p1], %[q1]\n\t"
              "s_andn2_b64 %[pm], %[pm], exec\n\t"
              "s_cbranch_scc0 .Lrd_done%=\n\t"
              "s_bcnt1_i32_b64 vcc_lo, %[pm]\n\t"
              "s_cmp_le_u32 vcc_lo, %[slow]\n\t"
              "s_cbranch_scc1 .Lrd_fast0_%=\n\t"
              "s_sleep %[gap]\n\t"
              ".Lrd_fast0_%=:\n\t"
              "s_mov_b64 exec, %[pm]\n\t"
              "buffer_load_dwordx4 v[128:131], %[o0], %[srd], 0 offen offset:0 sc1\n\t"
              "buffer_load_dwordx4 v[132:135], %[o0], %[srd], 0 offen offset:1024 sc1\n\t"
              "buffer_load_dwordx4 v[136:139], %[o0], %[srd], 0 offen offset:2048 sc1\n\t"
              "buffer_load_dwordx4 v[140:143], %[o0], %[srd], 0 offen offset:3072 sc1\n\t"
              "s_mov_b64 exec, %[sv]\n\t"
              "s_waitcnt vmcnt(11)\n\t"
              "v_bitop3_b32 %[e8], v144, v146, %[t8] bitop3:0x7e\n\t"
              "v_bitop3_b32 %[e32], v145, v147, %[t32] bitop3:0x7e\n\t"
              "v_and_b32 %[p0], %[msk], v144\n\t"
              "v_and_b32 %[p1], %[msk], v146\n\t"
              "s_waitcnt vmcnt(10)\n\t"
              "v_bitop3_b32 %[u8], v148, v150, %[t8] bitop3:0x7e\n\t"
              "v_bitop3_b32 %[u32], v149, v151, %[t32] bitop3:0x7e\n\t"
              "v_and_b32 %[q0], %[msk], v148\n\t"
              "v_and_b32 %[q1], %[msk], v150\n\t"
              "v_or_b32 %[e8], %[e8], %[u8]\n\t"
              "v_or_b32 %[e32], %[e32], %[u32]\n\t"
              "v_add_f32 %[p0], %[p0], %[q0]\n\t"
              "v_add_f32 %[p1], %[p1], %[q1]\n\t"
              "s_waitcnt vmcnt(9)\n\t"
              "v_bitop3_b32 %[u8], v152, v154, %[t8] bitop3:0x7e\n\t"
              "v_bitop3_b32 %[u32], v153, v155, %[t32] bitop3:0x7e\n\t"
              "v_and_b32 %[q0], %[msk], v152\n\t"
              "v_and_b32 %[q1], %[msk], v154\n\t"
              "v_or_b32 %[e8], %[e8], %[u8]\n\t"
              "v_or_b32 %[e32], %[e32], %[u32]\n\t"
              "v_add_f32 %[p0], %[p0], %[q0]\n\t"
              "v_add_f32 %[p1], %[p1], %[q1]\n\t"
              "s_waitcnt vmcnt(8)\n\t"
              "v_bitop3_b32 %[u8], v156, v158, %[t8] bitop3:0x7e\n\t"
              "v_bitop3_b32 %[u32], v157, v159, %[t32] bitop3:0x7e\n\t"
              "v_bitop3_b32 %[e8], %[e8], %[u8], %[ff] bitop3:0xa8\n\t"
              "v_or3_b32 %[e8], %[e8], %[e32], %[u32]\n\t"
              "v_cmp_eq_u32 vcc, 0, %[e8]\n\t"
              "v_and_b32 %[q0], %[msk], v156\n\t"
              "v_and_b32 %[q1], %[msk], v158\n\t"
              "s_and_b64 exec, vcc, %[pm]\n\t"
              "v_add_f32 %[v0], %[p0], %[q0]\n\t"
              "v_add_f32 %[v1], %[p1], %[q1]\n\t"
              "s_andn2_b64 %[pm], %[pm], exec\n\t"
              "s_cbranch_scc0 .Lrd_done%=\n\t"
              "s_bcnt1_i32_b64 vcc_lo, %[pm]\n\t"
              "s_cmp_le_u32 vcc_lo, %[slow]\n\t"
              "s_cbranch_scc1 .Lrd_fast1_%=\n\t"
              "s_sleep %[gap]\n\t"
              ".Lrd_fast1_%=:\n\t"
              "s_mov_b64 exec, %[pm]\n\t"
              "buffer_load_dwordx4 v[144:147], %[o0], %[srd], 0 offen offset:0 sc1\n\t"
              "buffer_load_dwordx4 v[148:151], %[o0], %[srd], 0 offen offset:1024 sc1\n\t"
              "buffer_load_dwordx4 v[152:155], %[o0], %[srd], 0 offen offset:2048 sc1\n\t"
              "buffer_load_dwordx4 v[156:159], %[o0], %[srd], 0 offen offset:3072 sc1\n\t"
              "s_mov_b64 exec, %[sv]\n\t"
              "s_waitcnt vmcnt(11)\n\t"
              "v_bitop3_b32 %[e8], v160, v162, %[t8] bitop3:0x7e\n\t"
              "v_bitop3_b32 %[e32], v161, v163, %[t32] bitop3:0x7e\n\t"
              "v_and_b32 %[p0], %[msk], v160\n\t"
              "v_and_b32 %[p1], %[msk], v162\n\t"
              "s_waitcnt vmcnt(10)\n\t"
              "v_bitop3_b32 %[u8], v164, v166, %[t8] bitop3:0x7e\n\t"
              "v_bitop3_b32 %[u32], v165, v167, %[t32] bitop3:0x7e\n\t"
              "v_and_b32 %[q0], %[msk], v164\n\t"
              "v_and_b32 %[q1], %[msk], v166\n\t"
              "v_or_b32 %[e8], %[e8], %[u8]\n\t"
              "v_or_b32 %[e32], %[e32], %[u32]\n\t"
              "v_add_f32 %[p0], %[p0], %[q0]\n\t"
              "v_add_f32 %[p1], %[p1], %[q1]\n\t"
              "s_waitcnt vmcnt(9)\n\t"
              "v_bitop3_b32 %[u8], v168, v170, %[t8] bitop3:0x7e\n\t"
              "v_bitop3_b32 %[u32], v169, v171, %[t32] bitop3:0x7e\n\t"
              "v_and_b32 %[q0], %[msk], v168\n\t"
              "v_and_b32 %[q1], %[msk], v170\n\t"
              "v_or_b32 %[e8], %[e8], %[u8]\n\t"
              "v_or_b32 %[e32], %[e32], %[u32]\n\t"
              "v_add_f32 %[p0], %[p0], %[q0]\n\t"
              "v_add_f32 %[p1], %[p1], %[q1]\n\t"
              "s_waitcnt vmcnt(8)\n\t"
              "v_bitop3_b32 %[u8], v172, v174, %[t8] bitop3:0x7e\n\t"
              "v_bitop3_b32 %[u32], v173, v175, %[t32] bitop3:0x7e\n\t"
              "v_bitop3_b32 %[e8], %[e8], %[u8], %[ff] bitop3:0xa8\n\t"
              "v_or3_b32 %[e8], %[e8], %[e32], %[u32]\n\t"
              "v_cmp_eq_u32 vcc, 0, %[e8]\n\t"
              "v_and_b32 %[q0], %[msk], v172\n\t"
              "v_and_b32 %[q1], %[msk], v174\n\t"
              "s_and_b64 exec, vcc, %[pm]\n\t"
              "v_add_f32 %[v0], %[p0], %[q0]\n\t"
              "v_add_f32 %[v1], %[p1], %[q1]\n\t"
              "s_andn2_b64 %[pm], %[pm], exec\n\t"
              "s_cbranch_scc0 .Lrd_done%=\n\t"
              "s_bcnt1_i32_b64 vcc_lo, %[pm]\n\t"
              "s_cmp_le_u32 vcc_lo, %[slow]\n\t"
              "s_cbranch_scc1 .Lrd_fast2_%=\n\t"
              "s_sleep %[gap]\n\t"
              ".Lrd_fast2_%=:\n\t"
              "s_mov_b64 exec, %[pm]\n\t"
              "buffer_load_dwordx4 v[160:163], %[o0], %[srd], 0 offen offset:0 sc1\n\t"
              "buffer_load_dwordx4 v[164:167], %[o0], %[srd], 0 offen offset:1024 sc1\n\t"
              "buffer_load_dwordx4 v[168:171], %[o0], %[srd], 0 offen offset:2048 sc1\n\t"
              "buffer_load_dwordx4 v[172:175], %[o0], %[srd], 0 offen offset:3072 sc1\n\t"
              "s_mov_b64 exec, %[sv]\n\t"
              "s_sub_u32 %[cnt], %[cnt], 1\n\ts_cbranch_scc0 .Lrd_loop%=\n\t"
              ".Lrd_done%=:\n\ts_mov_b64 exec, %[sv]\n\ts_nop 1"
              : "=&{v[128:131]}"(A0), "=&{v[132:135]}"(A1), "=&{v[136:139]}"(A2), "=&{v[140:143]}"(A3), "=&{v[144:147]}"(B0), "=&{v[148:151]}"(B1), "=&{v[152:155]}"(B2), "=&{v[156:159]}"(B3), "=&{v[160:163]}"(C0), "=&{v[164:167]}"(C1), "=&{v[168:171]}"(C2), "=&{v[172:175]}"(C3),
                [v0] "=&v"(v0), [v1] "=&v"(v1), [p0] "=&v"(p0), [p1] "=&v"(p1), [q0] "=&v"(q0), [q1] "=&v"(q1),
                [e8] "=&v"(e8), [e32] "=&v"(e32), [u8] "=&v"(u8), [u32] "=&v"(u32), [pm] "=&s"(pm), [sv] "=&s"(sv), [cnt] "=&s"(cnt)
              : [o0] "v"(off), [srd] "s"(srd), [t32] "s"(tag.t32), [t8] "s"(tag.t8), [ff] "s"(0xffu), [msk] "s"(0xffffff00u),
                [lim] "i"(SPIN_MAX), [gap] "i"(RGAP), [slow] "i"(RSLOW)
              : "vcc", "scc", "memory");
          STAMP(8);
          v0 += dpp<DPP_ROR4>(v0); v1 += dpp<DPP_ROR4>(v1);
          v0 += dpp<DPP_ROR8>(v0); v1 += dpp<DPP_ROR8>(v1);
          v0 = sum_rows(v0); v1 = sum_rows(v1);
          if (l < 4) {
            const int sg = (rw & 15) * 4 + l;
            *(GAS unsigned*)(uintptr_t)(out + (sg >> 5) * DM + (rw >> 4) * 64 + 8 * (sg & 7) + 4 * ((sg >> 4) & 1) + 2 * ((sg >> 3) & 1)) = pk_bf16(v0, v1);
            if (rw == 0 && l == 0) *(GAS u64*)(uintptr_t)(ws + WS_EPOCH) = ep;
          }
          asm volatile("s_waitcnt vmcnt(0)" :: "{v[128:131]}"(A0), "{v[132:135]}"(A1), "{v[136:139]}"(A2), "{v[140:143]}"(A3), "{v[144:147]}"(B0), "{v[148:151]}"(B1), "{v[152:155]}"(B2), "{v[156:159]}"(B3), "{v[160:163]}"(C0), "{v[164:167]}"(C1), "{v[168:171]}"(C2), "{v[172:175]}"(C3));
        }
      }
    } else if (isT) {
      lds_wait(&L.pvflag, 1);
      o_gather(w - 4);
    }
  }
  STAMP(9);
}

extern "C" void kernel_launch(void* const* d_in, const int* in_sizes, int n_in, void* d_out, int out_size, void* d_ws, size_t ws_size, hipStream_t stream) {
  fused_decode<<<256, 512, 0, stream>>>((const u16*)d_in[0], (const u16*)d_in[1], (const u16*)d_in[2], (const u16*)d_in[3], (const u16*)d_in[4],
                                        (const u16*)d_in[6], (const u16*)d_in[7], (const u16*)d_in[8], (const u16*)d_in[9], (u16*)d_out,
                                        (unsigned char*)d_ws);
}
```

```cpp
#include <hip/hip_runtime.h>
#include <stdint.h>

typedef unsigned short u16;
typedef unsigned long long u64;
typedef unsigned v16u __attribute__((ext_vector_type(16)));
typedef unsigned v4u __attribute__((ext_vector_type(4)));
typedef unsigned v3u __attribute__((ext_vector_type(3)));
typedef unsigned v2u __attribute__((ext_vector_type(2)));
typedef float v4f __attribute__((ext_vector_type(4)));
typedef float v2f __attribute__((ext_vector_type(2)));
typedef __bf16 bf2 __attribute__((ext_vector_type(2)));
typedef __bf16 bf8 __attribute__((ext_vector_type(8)));
typedef __amdgpu_buffer_rsrc_t rsrc_t;
#define GAS __attribute__((address_space(1)))
#define LAS __attribute__((address_space(3)))

constexpr int DM = 576, SK = 4096, FF = 4608;
constexpr int NP = 12, NPMIN = 9, NOPT = NP - NPMIN;
constexpr int AUX_NT = 2, AUX_SC1 = 16;
constexpr unsigned OOR = 0x80000000u;
#define ZI
constexpr int SPIN_MAX = 1 << 16;
#define NFA 64
#define NSA 59
#define N5A 72
#define N6A 55
#define NFB 66
#define NSB 60
#define N6B 56
#define NFC 63
#define NSC 58
#define N6C 72
#define KF 4
#define TQ 450
#define TQD 280
#define TH 850
#define PD 3
#define PGAP 0
#define RD 3
#define RGAP 6
#define RSLOW 16
constexpr bool okq(int n) { return n >= 6 * NPMIN && n <= 6 * NP; }
static_assert(okq(NFA) && okq(NSA) && okq(N5A) && okq(N6A) && okq(NFB) && okq(NSB) && okq(N6B) && okq(NFC) && okq(NSC) && okq(N6C) &&
              okq(512 - 4 * NFA - NSA - N5A - N6A) && okq(512 - 4 * NFB - 2 * NSB - N6B) && okq(512 - 4 * NFC - 2 * NSC - N6C), "token counts out of range");
constexpr unsigned WS_EPOCH = 0;
constexpr unsigned WS_G = 1024;
constexpr unsigned WS_QB = 4096;
constexpr unsigned WS_O1 = WS_QB + 16 * 16384;
constexpr unsigned WS_HB = WS_O1 + 16 * 16 * 2048;
constexpr unsigned WS_FB = WS_HB + 6 * FF * 16;
constexpr unsigned WS_END = WS_FB + 9 * 64 * 1024;

#define STAMP(k) do { } while (0)

__device__ __forceinline__ float as_f(unsigned u) { return __builtin_bit_cast(float, u); }
__device__ __forceinline__ unsigned as_u(float f) { return __builtin_bit_cast(unsigned, f); }
__device__ __forceinline__ bf2 as_bf2(unsigned u) { return __builtin_bit_cast(bf2, u); }
__device__ __forceinline__ bf8 as_bf8(v4u u) { return __builtin_bit_cast(bf8, u); }
__device__ __forceinline__ unsigned pk_bf16(float a, float b) { v2f p = {a, b}; return __builtin_bit_cast(unsigned, __builtin_convertvector(p, bf2)); }
__device__ __forceinline__ float rnd_bf16(float a) { return as_f(pk_bf16(a, a) & 0xffff0000u); }
template <int CTRL> __device__ __forceinline__ float dpp(float v) {
  return as_f((unsigned)__builtin_amdgcn_update_dpp(0, (int)as_u(v), CTRL, 0xf, 0xf, true));
}
constexpr int DPP_ROR8 = 0x128, DPP_ROR4 = 0x124, DPP_HMIRROR = 0x141, DPP_X1 = 0xB1, DPP_X2 = 0x4E;
__device__ __forceinline__ float sum8(float t) {
  t += dpp<DPP_HMIRROR>(t); t += dpp<DPP_X1>(t); t += dpp<DPP_X2>(t); return t;
}
__device__ __forceinline__ float max8(float t) {
  t = fmaxf(t, dpp<DPP_HMIRROR>(t)); t = fmaxf(t, dpp<DPP_X1>(t)); t = fmaxf(t, dpp<DPP_X2>(t)); return t;
}
__device__ __forceinline__ float sum_rows(float t) {
  auto a = __builtin_amdgcn_permlane16_swap(as_u(t), as_u(t), false, false);
  unsigned a0 = a[0], a1 = a[1];
  t = as_f(a0) + as_f(a1);
  auto b = __builtin_amdgcn_permlane32_swap(as_u(t), as_u(t), false, false);
  unsigned b0 = b[0], b1 = b[1];
  return as_f(b0) + as_f(b1);
}
__device__ __forceinline__ float max_rows(float t) {
  auto a = __builtin_amdgcn_permlane16_swap(as_u(t), as_u(t), false, false);
  unsigned a0 = a[0], a1 = a[1];
  t = fmaxf(as_f(a0), as_f(a1));
  auto b = __builtin_amdgcn_permlane32_swap(as_u(t), as_u(t), false, false);
  unsigned b0 = b[0], b1 = b[1];
  return fmaxf(as_f(b0), as_f(b1));
}
__device__ __forceinline__ rsrc_t mk_rsrc(const void* p, unsigned bytes) {
  return __builtin_amdgcn_make_buffer_rsrc(const_cast<void*>(p), 0, bytes, 0x00020000);
}
__device__ __forceinline__ v4u ld_nt(rsrc_t r, unsigned voff, unsigned soff) { return __builtin_amdgcn_raw_buffer_load_b128(r, voff, soff, AUX_NT); }
__device__ __forceinline__ v4u ld_sc(rsrc_t r, unsigned voff, unsigned soff) { return __builtin_amdgcn_raw_buffer_load_b128(r, voff, soff, AUX_SC1); }
#define AUXG AUX_SC1
__device__ __forceinline__ v4u ld_g(rsrc_t r, unsigned voff, unsigned soff) { return __builtin_amdgcn_raw_buffer_load_b128(r, voff, soff, AUXG); }
__device__ __forceinline__ void st_sc(v4u v, rsrc_t r, unsigned voff, unsigned soff) { __builtin_amdgcn_raw_buffer_store_b128(v, r, voff, soff, AUX_SC1); }
__device__ __forceinline__ v4f mfma(v4u a, v4u b, v4f c) {
  return __builtin_amdgcn_mfma_f32_16x16x32_bf16(as_bf8(a), as_bf8(b), c, 0, 0, 0);
}
struct Tag { unsigned t32, t8; };
__device__ __forceinline__ v4u gran2(float a, float b, Tag t) {
  v4u g;
  g.x = ((as_u(a) + 0x80u) & 0xffffff00u) | t.t8; g.y = t.t32;
  g.z = ((as_u(b) + 0x80u) & 0xffffff00u) | t.t8; g.w = t.t32;
  return g;
}
__device__ __forceinline__ unsigned gbad(v4u g, Tag t) {
  unsigned x = g.x, y = g.y, z = g.z, w = g.w;
  return (((x ^ t.t8) | (z ^ t.t8)) & 0xffu) | (y ^ t.t32) | (w ^ t.t32);
}
__device__ __forceinline__ float gv0(v4u g) { unsigned x = g.x; return as_f(x & 0xffffff00u); }
__device__ __forceinline__ float gv1(v4u g) { unsigned z = g.z; return as_f(z & 0xffffff00u); }
#define SB() __builtin_amdgcn_sched_barrier(0)
#define CBAR() asm volatile("" ::: "memory")
__device__ __forceinline__ void wg_barrier() {
  asm volatile("s_waitcnt lgkmcnt(0)" ::: "memory");
  __builtin_amdgcn_s_barrier();
  asm volatile("" ::: "memory");
}
__device__ __forceinline__ float bsel(unsigned m, float a, float b) { return as_f((as_u(a) & m) | (as_u(b) & ~m)); }
__device__ __forceinline__ unsigned dup_lo(unsigned p) { return __builtin_amdgcn_perm(p, p, 0x01000100u); }
__device__ __forceinline__ unsigned dup_hi(unsigned p) { return __builtin_amdgcn_perm(p, p, 0x03020302u); }

struct __attribute__((aligned(16))) Lds {
  unsigned P[6][8][2 * NP];
  float ow[6][256];
  float ml[6][4];
  float fu[6][256];
  unsigned qs[2][64];
  unsigned tabo[4][2][2][4][4];
  unsigned tabh[2][4][12];
  float gs[2];
  v2f hx[448];
  unsigned qflag, fcnt, ocnt, hflag, pvflag, vflag[3], mcnt, acnt, qsent;
};
__device__ __forceinline__ unsigned lds_ld(unsigned* p) {
  return __builtin_amdgcn_readfirstlane(__hip_atomic_load(p, __ATOMIC_RELAXED, __HIP_MEMORY_SCOPE_WORKGROUP));
}
__device__ __forceinline__ void lds_wait(unsigned* p, unsigned v) {
#pragma nounroll
  for (int spin = 0; spin < SPIN_MAX * 16; ++spin) {
    CBAR();
    if (lds_ld(p) >= v) break;
    __builtin_amdgcn_s_sleep(1);
  }
  CBAR();
}
__device__ __forceinline__ void lds_set(unsigned* p) {
  asm volatile("s_waitcnt lgkmcnt(0)" ::: "memory");
  __hip_atomic_store(p, 1u, __ATOMIC_RELAXED, __HIP_MEMORY_SCOPE_WORKGROUP);
  CBAR();
}
__device__ __forceinline__ void lds_inc(unsigned* p, int l) {
  asm volatile("s_waitcnt lgkmcnt(0)" ::: "memory");
  if (l == 0) __hip_atomic_fetch_add(p, 1u, __ATOMIC_RELAXED, __HIP_MEMORY_SCOPE_WORKGROUP);
  CBAR();
}
__device__ __forceinline__ void lds_inc_nw(unsigned* p, int l) {
  CBAR();
  if (l == 0) __hip_atomic_fetch_add(p, 1u, __ATOMIC_RELAXED, __HIP_MEMORY_SCOPE_WORKGROUP);
  CBAR();
}
__device__ __forceinline__ unsigned lds_addr(const void* p) { return (unsigned)(uintptr_t)(const __attribute__((address_space(3))) void*)p; }
__device__ __forceinline__ void wait_until(u64 t) {
#pragma nounroll
  for (int spin = 0; spin < 4096; ++spin) {
    if (__builtin_amdgcn_s_memrealtime() >= t) break;
    __builtin_amdgcn_s_sleep(4);
  }
}
__device__ __forceinline__ float swap16_sum(float t) {
  auto a = __builtin_amdgcn_permlane16_swap(as_u(t), as_u(t), false, false);
  unsigned a0 = a[0], a1 = a[1];
  return as_f(a0) + as_f(a1);
}
__device__ __forceinline__ float swap16_max(float t) {
  auto a = __builtin_amdgcn_permlane16_swap(as_u(t), as_u(t), false, false);
  unsigned a0 = a[0], a1 = a[1];
  return fmaxf(as_f(a0), as_f(a1));
}
__device__ __forceinline__ float swap32_sum(float t) {
  auto a = __builtin_amdgcn_permlane32_swap(as_u(t), as_u(t), false, false);
  unsigned a0 = a[0], a1 = a[1];
  return as_f(a0) + as_f(a1);
}
__device__ __forceinline__ float swap32_max(float t) {
  auto a = __builtin_amdgcn_permlane32_swap(as_u(t), as_u(t), false, false);
  unsigned a0 = a[0], a1 = a[1];
  return fmaxf(as_f(a0), as_f(a1));
}

constexpr float SCL = 0.08838834764831845f * 1.4426950408889634f;
__device__ __forceinline__ void ld_opt(v4u (&r)[2 * NP], rsrc_t rs, unsigned lo, int np) {
#pragma unroll
  for (int tp = NPMIN; tp < NP; ++tp) {
    const int rp = tp - NPMIN;
    r[2 * rp] = v4u{0, 0, 0, 0}; r[2 * rp + 1] = v4u{0, 0, 0, 0};
    if (np > tp) { r[2 * rp] = ld_nt(rs, lo + (tp & 1) * 2048, (tp >> 1) * 4096); r[2 * rp + 1] = ld_nt(rs, lo + (tp & 1) * 2048 + 1024, (tp >> 1) * 4096); }
  }
  SB();
}
template <int T0, int T1>
__device__ __forceinline__ void ld_fix(v4u (&r)[2 * NP], rsrc_t rs, unsigned lo) {
#pragma unroll
  for (int tp = T0; tp < T1; ++tp) {
    const int rp = tp + NOPT;
    r[2 * rp] = ld_nt(rs, lo + (tp & 1) * 2048, (tp >> 1) * 4096);
    SB();
    r[2 * rp + 1] = ld_nt(rs, lo + (tp & 1) * 2048 + 1024, (tp >> 1) * 4096);
    SB();
  }
}
constexpr int vmcnt_imm(int n) { return (n & 15) | (7 << 4) | (15 << 8) | ((n >> 4) << 14); }

__global__ void __launch_bounds__(512, 2)
fused_decode(const u16* x_, const u16* kc_, const u16* vc_, const u16* wq_, const u16* wo_, const u16* wg_, const u16* wu_,
             const u16* wd_, const u16* gw_, u16* out_, unsigned char* ws_) {
  __shared__ Lds L;
  v16u ka; v4u kb; v2u kd;
  int bid = blockIdx.x;
  asm volatile("s_load_dwordx16 %0, %4, 0x0\n\ts_load_dwordx4 %1, %4, 0x40\n\ts_load_dwordx2 %2, %4, 0x50"
               : "=&s"(ka), "=&s"(kb), "=&s"(kd), "+s"(bid) : "s"(__builtin_amdgcn_kernarg_segment_ptr()));
  SB();
  const int tid = threadIdx.x, l = tid & 63;
  const int w = __builtin_amdgcn_readfirstlane(tid >> 6);
  bid = (bid >> 6 << 6) | ((bid & 1) << 5) | (((bid >> 4) & 3) << 3) | (bid & 6) | ((bid >> 3) & 1);
  const int xc = bid & 7, slot = bid >> 3, p4 = slot >> 3;
  const int kvh = 2 * xc + (slot >> 4), b = p4 & 1, s = slot & 7, j = slot & 15;
  const int pr = xc * 4 + p4;
  const bool isT = s < 6;
  const bool isF = (s < 5) | ((s == 5) & (pr >= 3)) | ((s == 6) & (pr < 27));
  const unsigned tu = 6 * pr + s - min(pr, 3), tu7 = (tu * 9363u) >> 16;
  const int frest = s == 6 ? pr : (int)tu7, flo = s == 6 ? 0 : (int)(1 + tu - 7 * tu7);
  const int xg = xc >> 1, cx = xg == 3 ? 0 : xg, cn = cx + 1 + (s & 1);
  const int rg = s < 4 ? b * 2 + (s >> 1) : b * 2 + (s & 1), cg = s < 4 ? (cn > 2 ? cn - 3 : cn) : cx;
  const int kvt = (kvh + 4) & 15, R = kvt * 4 + rg;
  const int g = l >> 4, c = l & 15, e = l & 7, hb = (l >> 3) & 1;
  STAMP(0);
  const u64 tstart = __builtin_amdgcn_s_memrealtime();
  const int nF = pr < 3 ? NFA : (pr < 27 ? NFB : NFC), nS = pr < 3 ? NSA : (pr < 27 ? NSB : NSC), n5 = pr < 3 ? N5A : nS;
  const int n6 = pr < 3 ? N6A : (pr < 27 ? N6B : N6C);
  const int wg0 = min(s, 4) * nF + (s > 4 ? nS : 0) + (s > 5 ? n5 : 0) + (s > 6 ? n6 : 0);
  const int nq = s < 4 ? nF : (s == 4 ? nS : (s == 5 ? n5 : (s == 6 ? n6 : 512 - wg0)));
  const int wi = w < 4 ? w - 1 : w - 2;
  const int nb = (nq * 43) >> 8, nr = nq - 6 * nb;
  int np = nb + (wi < nr ? 1 : 0);
  unsigned kvq = (b * 16 + kvh) * 512 + wg0 + wi * nb + min(wi, nr);
  int fks = (frest * 57) >> 9;
  int fhi = frest - 9 * fks;
  v4u dm;
  {
    const unsigned hm = (e & 1) ? 0xffff0000u : 0x0000ffffu;
    dm.x = (e >> 1) == 0 ? hm : 0u; dm.y = (e >> 1) == 1 ? hm : 0u; dm.z = (e >> 1) == 2 ? hm : 0u; dm.w = (e >> 1) == 3 ? hm : 0u;
  }
  int flo3 = flo + 3;
  asm volatile("" : "+s"(np), "+s"(kvq), "+s"(fhi), "+s"(flo3), "+s"(fks));
  SB();
  asm volatile("s_waitcnt lgkmcnt(0)" : "+s"(ka), "+s"(kb), "+s"(kd));
  SB();
  STAMP(7);
#define KPTR(lo, hi) (((u64)(hi) << 32) | (lo))
  const u16* x = (const u16*)KPTR(ka[0], ka[1]);
  const u16* kc = (const u16*)KPTR(ka[2], ka[3]);
  const u16* vc = (const u16*)KPTR(ka[4], ka[5]);
  const u16* wq = (const u16*)KPTR(ka[6], ka[7]);
  const u16* wo = (const u16*)KPTR(ka[8], ka[9]);
  const u16* wg = (const u16*)KPTR(ka[10], ka[11]);
  const u16* wu = (const u16*)KPTR(ka[12], ka[13]);
  const u16* wd = (const u16*)KPTR(ka[14], ka[15]);
  const u16* gw = (const u16*)KPTR(kb[0], kb[1]);
  u16* out = (u16*)KPTR(kb[2], kb[3]);
  unsigned char* ws = (unsigned char*)KPTR(kd[0], kd[1]);
  const int fcbg = 8 * fhi + ((flo3 - (int)((uintptr_t)wg >> 7)) & 7);
  const int fcbu = 8 * fhi + ((flo3 - (int)((uintptr_t)wu >> 7)) & 7);
  const rsrc_t rws = mk_rsrc(ws, WS_END), rx = mk_rsrc(x, 2 * DM * 2);
  const v4u srd = {kd[0], kd[1] & 0xffffu, WS_END, 0x00020000u};
  u64 ep;
  Tag tag;
#define LOAD_EPOCH() do { const unsigned char* p_ = ws; asm volatile("" : "+s"(p_) :: "memory"); ep = *(const __attribute__((address_space(4))) u64*)(uintptr_t)(p_ + WS_EPOCH); } while (0)
#define PIN_EPOCH() do { SB(); asm volatile("" : "+s"(ep) :: "memory"); ep += 1ull; tag.t32 = (unsigned)ep; tag.t8 = (unsigned)(ep >> 32) & 0xffu; } while (0)
  auto o_gather = [&](int bw) __attribute__((always_inline)) {
    const unsigned off = WS_O1 + (kvt * 16 + (l >> 2)) * 2048 + rg * 512 + bw * 128 + (l & 3) * 16;
    const unsigned ps0 = (l & 4) ? 0x01000c0cu : 0x0c0c0100u, ps1 = (l & 4) ? 0x03020c0cu : 0x0c0c0302u;
    const unsigned tp = lds_addr(&L.tabo[2 * hb + (bw >> 1)][(l >> 2) & 1][l >> 5][l & 3][2 * (bw & 1)]);
    lds_wait(&L.hflag, 1);
    const float gsb = L.gs[l >> 5];
    unsigned e0, e1, cnt;
    u64 sv;
    asm volatile(
        "s_mov_b64 %[sv], exec\n\ts_mov_b32 %[cnt], %[lim]\n\t"
        ".Log_loop%=:\n\t"
        "buffer_load_dwordx4 v[128:131], %[o0], %[srd], 0 offen sc1\n\t"
        "buffer_load_dwordx4 v[132:135], %[o0], %[srd], 0 offen offset:64 sc1\n\t"
        "s_waitcnt vmcnt(1)\n\t"
        "v_bitop3_b32 %[e0], v129, v131, %[t32] bitop3:0x7e\n\t"
        "s_waitcnt vmcnt(0)\n\t"
        "v_bitop3_b32 %[e1], v133, v135, %[t32] bitop3:0x7e\n\t"
        "v_or_b32 %[e0], %[e0], %[e1]\n\t"
        "v_cmp_ne_u32 vcc, 0, %[e0]\n\t"
        "s_and_b64 exec, exec, vcc\n\t"
        "s_cbranch_scc0 .Log_done%=\n\t"
        "s_sleep %[gap]\n\t"
        "s_sub_u32 %[cnt], %[cnt], 1\n\ts_cbranch_scc0 .Log_loop%=\n\t"
        ".Log_done%=:\n\ts_mov_b64 exec, %[sv]\n\t"
        "v_perm_b32 v136, v130, v128, %[s0]\n\t"
        "v_perm_b32 v137, v134, v132, %[s1]\n\t"
        "v_or_b32 v136, v136, v137\n\t"
        "s_nop 1\n\t"
        "v_max_f32_dpp v137, v136, v136 row_ror:4 row_mask:0xf bank_mask:0xf\n\t"
        "s_nop 1\n\t"
        "v_max_f32_dpp v138, v137, v137 row_ror:8 row_mask:0xf bank_mask:0xf\n\t"
        "v_mov_b32 v139, v138\n\t"
        "s_nop 1\n\t"
        "v_permlane16_swap_b32 v138, v139\n\t"
        "v_max_f32 v138, v138, v139\n\t"
        "v_sub_f32 v138, v136, v138\n\t"
        "v_exp_f32 v138, v138\n\t"
        "s_nop 1\n\t"
        "v_mul_f32_dpp v136, v138, v136 quad_perm:[0,0,2,2] row_mask:0xf bank_mask:0xf\n\t"
        "v_mul_f32_dpp v128, v138, v128 quad_perm:[0,0,2,2] row_mask:0xf bank_mask:0xf\n\t"
        "v_mul_f32_dpp v130, v138, v130 quad_perm:[0,0,2,2] row_mask:0xf bank_mask:0xf\n\t"
        "v_mul_f32_dpp v132, v138, v132 quad_perm:[0,0,2,2] row_mask:0xf bank_mask:0xf\n\t"
        "v_mul_f32_dpp v134, v138, v134 quad_perm:[0,0,2,2] row_mask:0xf bank_mask:0xf\n\t"
        "v_add_f32_dpp v137, v136, v136 row_ror:4 row_mask:0xf bank_mask:0xf\n\t"
        "v_add_f32_dpp v129, v128, v128 row_ror:4 row_mask:0xf bank_mask:0xf\n\t"
        "v_add_f32_dpp v131, v130, v130 row_ror:4 row_mask:0xf bank_mask:0xf\n\t"
        "v_add_f32_dpp v133, v132, v132 row_ror:4 row_mask:0xf bank_mask:0xf\n\t"
        "v_add_f32_dpp v135, v134, v134 row_ror:4 row_mask:0xf bank_mask:0xf\n\t"
        "v_add_f32_dpp v136, v137, v137 row_ror:8 row_mask:0xf bank_mask:0xf\n\t"
        "v_add_f32_dpp v140, v129, v129 row_ror:8 row_mask:0xf bank_mask:0x3\n\t"
        "v_add_f32_dpp v140, v131, v131 row_ror:8 row_mask:0xf bank_mask:0xc\n\t"
        "v_add_f32_dpp v141, v133, v133 row_ror:8 row_mask:0xf bank_mask:0x3\n\t"
        "v_add_f32_dpp v141, v135, v135 row_ror:8 row_mask:0xf bank_mask:0xc\n\t"
        "v_mov_b32 v137, v136\n\t"
        "v_mov_b32 v142, v140\n\t"
        "v_mov_b32 v143, v141\n\t"
        "v_permlane16_swap_b32 v136, v137\n\t"
        "v_permlane16_swap_b32 v140, v142\n\t"
        "v_permlane16_swap_b32 v141, v143\n\t"
        "v_add_f32 v136, v136, v137\n\t"
        "v_rcp_f32 v136, v136\n\t"
        "v_pk_add_f32 v[140:141], v[140:141], v[142:143]\n\t"
        "s_nop 0\n\t"
        "v_mul_f32_dpp v138, v136, %[gs] quad_perm:[1,1,3,3] row_mask:0xf bank_mask:0xf\n\t"
        "v_pk_mul_f32 v[140:141], v[140:141], v[138:139] op_sel_hi:[1,0]\n\t"
        "v_cvt_pk_bf16_f32 v138, v140, v141\n\t"
        "v_perm_b32 v140, v138, v138, %[p0]\n\t"
        "v_perm_b32 v141, v138, v138, %[p1]\n\t"
        "ds_write_b64 %[tp], v[140:141]"
        : [e0] "=&v"(e0), [e1] "=&v"(e1), [sv] "=&s"(sv), [cnt] "=&s"(cnt)
        : [o0] "v"(off), [srd] "s"(srd), [t32] "s"(tag.t32), [lim] "i"(SPIN_MAX), [gap] "i"(PGAP), [s0] "s"(0x0c0c0400u), [s1] "s"(0x04000c0cu),
          [gs] "v"(gsb), [p0] "v"(ps0), [p1] "v"(ps1), [tp] "v"(tp)
        : "vcc", "scc", "memory", "v128", "v129", "v130", "v131", "v132", "v133", "v134", "v135", "v136", "v137", "v138", "v139", "v140", "v141", "v142", "v143");
    STAMP(10);
    wg_barrier();
  };
  if (w == 0) {
    __builtin_amdgcn_s_setprio(3);
    const int qcb = 8 * (2 * p4 + (xc & 1)) + ((s + 4 - (int)((uintptr_t)wq >> 7)) & 7), kq = xc >> 1;
    unsigned xa[2][9];
    v4u wr[18], gx[3], gg[3];
    {
      const rsrc_t rq = mk_rsrc(wq + (size_t)kq * 144 * 4096 + qcb * 64, 144 * 8192);
      const unsigned vo = (72 * hb + 18 * g) * 8192 + e * 16;
#pragma unroll
      for (int r = 0; r < 2; ++r) {
        const unsigned xo = (hb * DM + kq * 144 + 72 * r + 18 * g) * 2;
        v4u t0 = __builtin_amdgcn_raw_buffer_load_b128(rx, xo, 0, 0), t1 = __builtin_amdgcn_raw_buffer_load_b128(rx, xo + 16, 0, 0);
        xa[r][0] = t0.x; xa[r][1] = t0.y; xa[r][2] = t0.z; xa[r][3] = t0.w;
        xa[r][4] = t1.x; xa[r][5] = t1.y; xa[r][6] = t1.z; xa[r][7] = t1.w;
        xa[r][8] = __builtin_amdgcn_raw_buffer_load_b32(rx, xo + 32, 0, 0);
      }
      SB();
#pragma unroll
      for (int i = 0; i < 18; ++i) { wr[i] = ld_nt(rq, vo, i * 8192); SB(); }
    }
    __builtin_amdgcn_s_barrier();
    LOAD_EPOCH();
    if (s == 6) {
      const rsrc_t rg_ = mk_rsrc(gw, DM * 2);
#pragma unroll
      for (int i = 0; i < 3; ++i) {
        const int ck = (l & 31) + 32 * i;
        gg[i] = __builtin_amdgcn_raw_buffer_load_b128(rg_, ck * 16, 0, 0);
        gx[i] = __builtin_amdgcn_raw_buffer_load_b128(rx, ((l >> 5) * 72 + ck) * 16, 0, 0);
      }
    }
    SB();
    PIN_EPOCH();
    {
      v4f d0 = {0, 0, 0, 0}, d1 = {0, 0, 0, 0};
#pragma unroll
      for (int i = 0; i < 18; ++i) {
        const unsigned p0 = (i & 1) ? dup_hi(xa[0][i >> 1]) : dup_lo(xa[0][i >> 1]);
        const unsigned p1 = (i & 1) ? dup_hi(xa[1][i >> 1]) : dup_lo(xa[1][i >> 1]);
        d0 = mfma(dm & p0, wr[i], d0);
        d1 = mfma(dm & p1, wr[i], d1);
      }
      float r0 = hb ? d1.x : d0.x, r1 = hb ? d1.y : d0.y, r2 = hb ? d1.z : d0.z, r3 = hb ? d1.w : d0.w;
      r0 += dpp<DPP_ROR8>(r0); r1 += dpp<DPP_ROR8>(r1); r2 += dpp<DPP_ROR8>(r2); r3 += dpp<DPP_ROR8>(r3);
      st_sc(gran2(hb ? r2 : r0, hb ? r3 : r1, tag), rws, WS_QB + (((qcb >> 2) * 2 + (l >> 5)) * 4 + kq) * 2048 + (qcb & 3) * 512 + (l & 31) * 16, 0);
      SB();
      lds_inc_nw(&L.qsent, l);
    }
    STAMP(1);
    if (s == 6) {
      float gs = 0.f;
#pragma unroll
      for (int i = 0; i < 3; ++i) {
        const unsigned a0 = gg[i].x, a1 = gg[i].y, a2 = gg[i].z, a3 = gg[i].w, b0 = gx[i].x, b1 = gx[i].y, b2 = gx[i].z, b3 = gx[i].w;
        gs = __builtin_amdgcn_fdot2_f32_bf16(as_bf2(a0), as_bf2(b0), gs, false);
        gs = __builtin_amdgcn_fdot2_f32_bf16(as_bf2(a1), as_bf2(b1), gs, false);
        gs = __builtin_amdgcn_fdot2_f32_bf16(as_bf2(a2), as_bf2(b2), gs, false);
        gs = __builtin_amdgcn_fdot2_f32_bf16(as_bf2(a3), as_bf2(b3), gs, false);
      }
      gs = sum8(gs); gs += dpp<DPP_ROR8>(gs); gs = swap16_sum(gs);
      gs = 1.f / (1.f + __expf(-gs));
      st_sc(gran2(gs, as_f(__builtin_amdgcn_readlane(as_u(gs), 32)), tag), rws, l == 0 ? WS_G + pr * 64 : OOR, 0);
    }
    SB();
    if (isF) {
      lds_wait(&L.fcnt, 6);
      STAMP(2);
      const int ch = l >> 3;
      float ga[2] = {0.f, 0.f}, up[2] = {0.f, 0.f};
#pragma unroll
      for (int bb = 0; bb < 2; ++bb)
#pragma unroll
        for (int ww = 0; ww < 6; ++ww) { ga[bb] += L.fu[ww][ch * 16 + bb * 8 + e]; up[bb] += L.fu[ww][(8 + ch) * 16 + bb * 8 + e]; }
      const unsigned off = WS_HB + (fks * 2 * FF + l) * 16;
      st_sc(gran2(ga[0], ga[1], tag), rws, off, fcbg * 1024);
      st_sc(gran2(up[0], up[1], tag), rws, off, FF * 16 + fcbu * 1024);
      SB();
      STAMP(3);
    }
    lds_wait(&L.mcnt, 6);
    {
      float m0 = -INFINITY, m1 = -INFINITY;
      v4f mm[6];
#pragma unroll
      for (int ww = 0; ww < 6; ++ww) { mm[ww] = *(const v4f*)&L.ml[ww][0]; m0 = fmaxf(m0, mm[ww].x); m1 = fmaxf(m1, mm[ww].z); }
      v2f t0 = {0.f, 0.f}, t1 = {0.f, 0.f};
      float l0 = 0.f, l1 = 0.f, f0[6], f1[6];
#pragma unroll
      for (int ww = 0; ww < 6; ++ww) {
        f0[ww] = __builtin_amdgcn_exp2f(mm[ww].x - m0); f1[ww] = __builtin_amdgcn_exp2f(mm[ww].z - m1);
        l0 += f0[ww] * mm[ww].y; l1 += f1[ww] * mm[ww].w;
      }
      unsigned off = WS_O1 + (kvh * 16 + j) * 2048 + l * 16;
      const unsigned sh = (l & 4) * 4;
      unsigned u0 = as_u((l & 1) ? l0 : m0) >> sh, u1 = as_u((l & 1) ? l1 : m1) >> sh;
      lds_wait(&L.acnt, 3);
#pragma unroll
      for (int ww = 0; ww < 3; ++ww) { t0 += f0[ww] * *(const v2f*)&L.ow[ww][2 * l]; t1 += f1[ww] * *(const v2f*)&L.ow[ww][128 + 2 * l]; }
      asm volatile("" : "+v"(t0), "+v"(t1), "+v"(u0), "+v"(u1), "+v"(off), "+v"(f0[3]), "+v"(f0[4]), "+v"(f0[5]), "+v"(f1[3]), "+v"(f1[4]), "+v"(f1[5]));
      v4f a[3];
      {
        unsigned cv, cs, cnt;
        asm volatile(
            "s_mov_b32 %[cnt], %[lim]\n\t"
            ".Lpb_loop%=:\n\t"
            "ds_read_b32 %[cv], %[ca]\n\t"
            "ds_read2st64_b64 %[a0], %[ad] offset0:0 offset1:1\n\t"
            "ds_read2st64_b64 %[a1], %[ad] offset0:2 offset1:3\n\t"
            "ds_read2st64_b64 %[a2], %[ad] offset0:4 offset1:5\n\t"
            "s_waitcnt lgkmcnt(3)\n\t"
            "v_readfirstlane_b32 %[cs], %[cv]\n\t"
            "s_cmp_ge_u32 %[cs], 3\n\t"
            "s_cbranch_scc1 .Lpb_done%=\n\t"
            "s_sub_u32 %[cnt], %[cnt], 1\n\t"
            "s_cbranch_scc0 .Lpb_loop%=\n\t"
            ".Lpb_done%=:\n\t"
            "s_waitcnt lgkmcnt(0)"
            : [a0] "=&v"(a[0]), [a1] "=&v"(a[1]), [a2] "=&v"(a[2]), [cv] "=&v"(cv), [cs] "=&s"(cs), [cnt] "=&s"(cnt)
            : [ca] "v"(lds_addr(&L.ocnt)), [ad] "v"(lds_addr(&L.ow[3][2 * l])), [lim] "i"(SPIN_MAX * 16)
            : "scc", "memory");
      }
      STAMP(4);
#pragma unroll
      for (int ww = 0; ww < 3; ++ww) { t0 += f0[3 + ww] * a[ww].xy; t1 += f1[3 + ww] * a[ww].zw; }
      const v4u g0 = {__builtin_amdgcn_perm(as_u(t0.x), u0, 0x07060500u), tag.t32, __builtin_amdgcn_perm(as_u(t0.y), u0, 0x07060501u), tag.t32};
      const v4u g1 = {__builtin_amdgcn_perm(as_u(t1.x), u1, 0x07060500u), tag.t32, __builtin_amdgcn_perm(as_u(t1.y), u1, 0x07060501u), tag.t32};
      st_sc(g0, rws, off, 0);
      st_sc(g1, rws, off + 1024, 0);
      lds_set(&L.pvflag);
      STAMP(5);
    }
    if (isT) wg_barrier();
  } else if (w == 4) {
    __builtin_amdgcn_s_setprio(3);
    if (l < 11) (&L.qflag)[l] = 0;
    wg_barrier();
    LOAD_EPOCH();
    PIN_EPOCH();
    u64 tref = tstart;
    {
      const unsigned qoff = WS_QB + (kvh * 2 + b) * 8192 + l * 16;
      const unsigned qoff2 = qoff + 4096;
      v4u qa[8];
      lds_wait(&L.qsent, 1);
      { const u64 tq = __builtin_amdgcn_s_memrealtime() + (TQD - TQ); tref = tq > tref ? tq : tref; }
      wait_until(tref + TQ);
      STAMP(3);
      asm volatile(
          "buffer_load_dwordx4 %0, %[a0], %[srd], 0 offen offset:0 sc1\n\t"
          "buffer_load_dwordx4 %1, %[a0], %[srd], 0 offen offset:1024 sc1\n\t"
          "buffer_load_dwordx4 %2, %[a0], %[srd], 0 offen offset:2048 sc1\n\t"
          "buffer_load_dwordx4 %3, %[a0], %[srd], 0 offen offset:3072 sc1\n\t"
          "buffer_load_dwordx4 %4, %[a1], %[srd], 0 offen offset:0 sc1\n\t"
          "buffer_load_dwordx4 %5, %[a1], %[srd], 0 offen offset:1024 sc1\n\t"
          "buffer_load_dwordx4 %6, %[a1], %[srd], 0 offen offset:2048 sc1\n\t"
          "buffer_load_dwordx4 %7, %[a1], %[srd], 0 offen offset:3072 sc1"
          : "=&v"(qa[0]), "=&v"(qa[1]), "=&v"(qa[2]), "=&v"(qa[3]), "=&v"(qa[4]), "=&v"(qa[5]), "=&v"(qa[6]), "=&v"(qa[7])
          : [a0] "v"(qoff), [a1] "v"(qoff2), [srd] "s"(srd) : "memory");
      STAMP(11);
      asm volatile("s_waitcnt vmcnt(0)" : "+v"(qa[0]), "+v"(qa[1]), "+v"(qa[2]), "+v"(qa[3]), "+v"(qa[4]), "+v"(qa[5]), "+v"(qa[6]), "+v"(qa[7]));
      unsigned pend = 0xff;
#pragma nounroll
      for (int spin = 0; spin < SPIN_MAX; ++spin) {
#pragma unroll
        for (int i = 0; i < 8; ++i) if (__builtin_amdgcn_ballot_w64(gbad(qa[i], tag) != 0) == 0) pend &= ~(1u << i);
        if (pend == 0) break;
        __builtin_amdgcn_s_sleep(2);
        CBAR();
#pragma unroll
        for (int i = 0; i < 8; ++i) if (pend >> i & 1) qa[i] = ld_g(rws, qoff, i * 1024);
      }
      const int pi = (l >> 5) * 32 + 4 * e + 2 * (g & 1) + hb;
#pragma unroll
      for (int h = 0; h < 2; ++h) {
        const float a0 = (gv0(qa[h]) + gv0(qa[2 + h])) + (gv0(qa[4 + h]) + gv0(qa[6 + h]));
        const float a1 = (gv1(qa[h]) + gv1(qa[2 + h])) + (gv1(qa[4 + h]) + gv1(qa[6 + h]));
        L.qs[h][pi] = pk_bf16(a0, a1);
      }
      lds_set(&L.qflag);
    }
    STAMP(2);
    if (isT) {
      unsigned ho[7];
#pragma unroll
      for (int i = 0; i < 7; ++i) {
        const unsigned gi = 64 * i + l, km = (gi * 911u) >> 16;
        ho[i] = WS_HB + (km * (FF - 72) + R * 72 + gi) * 16;
      }
      ho[6] = l < 48 ? ho[6] : (l == 48 ? WS_G + pr * 64 : OOR);
      v4u a[7];
      wait_until(tref + TH);
      STAMP(4);
      asm volatile(
          "buffer_load_dwordx4 %0, %7, %[srd], 0 offen sc1\n\t"
          "buffer_load_dwordx4 %1, %8, %[srd], 0 offen sc1\n\t"
          "buffer_load_dwordx4 %2, %9, %[srd], 0 offen sc1\n\t"
          "buffer_load_dwordx4 %3, %10, %[srd], 0 offen sc1\n\t"
          "buffer_load_dwordx4 %4, %11, %[srd], 0 offen sc1\n\t"
          "buffer_load_dwordx4 %5, %12, %[srd], 0 offen sc1\n\t"
          "buffer_load_dwordx4 %6, %13, %[srd], 0 offen sc1"
          : "=&v"(a[0]), "=&v"(a[1]), "=&v"(a[2]), "=&v"(a[3]), "=&v"(a[4]), "=&v"(a[5]), "=&v"(a[6])
          : "v"(ho[0]), "v"(ho[1]), "v"(ho[2]), "v"(ho[3]), "v"(ho[4]), "v"(ho[5]), "v"(ho[6]), [srd] "s"(srd) : "memory");
      asm volatile("s_waitcnt vmcnt(0)" : "+v"(a[0]), "+v"(a[1]), "+v"(a[2]), "+v"(a[3]), "+v"(a[4]), "+v"(a[5]), "+v"(a[6]));
      unsigned pend = 0x7f;
#pragma nounroll
      for (int spin = 0; spin < SPIN_MAX; ++spin) {
#pragma unroll
        for (int i = 0; i < 7; ++i)
          if (__builtin_amdgcn_ballot_w64(gbad(a[i], tag) != 0 && (i < 6 || l <= 48)) == 0) pend &= ~(1u << i);
        if (pend == 0) break;
        __builtin_amdgcn_s_sleep(2);
        CBAR();
#pragma unroll
        for (int i = 0; i < 7; ++i) if (pend >> i & 1) a[i] = ld_g(rws, ho[i], 0);
      }
#pragma unroll
      for (int i = 0; i < 7; ++i) { v2f t = {gv0(a[i]), gv1(a[i])}; L.hx[64 * i + l] = t; }
      CBAR();
      const v2f gt = L.hx[432];
      if (l < 2) L.gs[l] = l ? gt.y : gt.x;
#pragma unroll
      for (int r = 0; r < 2; ++r) {
        const int rho = r ? 64 + e : l;
        v2f p[6];
#pragma unroll
        for (int i = 0; i < 6; ++i) p[i] = L.hx[72 * i + rho];
#pragma unroll
        for (int bb = 0; bb < 2; ++bb) {
          const float ga = rnd_bf16((p[0][bb] + p[2][bb]) + p[4][bb]);
          const float up = rnd_bf16((p[1][bb] + p[3][bb]) + p[5][bb]);
          const float si = rnd_bf16(ga / (1.f + __expf(-ga)));
          const float hv = rnd_bf16(si * up) * (1.f - gt[bb]);
          ((u16*)&L.tabh[bb][(rho >> 1) & 3][rho >> 3])[rho & 1] = (u16)pk_bf16(hv, hv);
        }
      }
      lds_set(&L.hflag);
      STAMP(5);
      lds_wait(&L.pvflag, 1);
      o_gather(0);
    }
  } else {
    __builtin_amdgcn_s_setprio(3);
    const size_t kvoff = (size_t)kvq * 1024;
    const rsrc_t rk = mk_rsrc(kc + kvoff, np * 2048), rv = mk_rsrc(vc + kvoff, np * 2048);
    const unsigned ko = ((l >> 2) & 3) * 256 + g * 64 + (l & 3) * 16;
#define PRE 0
    v4u kr[2 * NP], vr[2 * NP];
    ld_fix<0, PRE>(kr, rk, ko);
    wg_barrier();
    STAMP(8);
    {
      v4u fw[8], xf;
      if (isF) {
        xf = __builtin_amdgcn_raw_buffer_load_b128(rx, (hb * DM + fks * 192 + 32 * wi + 8 * g) * 2, 0, 0);
        const u16* fp = (hb ? wu + fcbu * 64 : wg + fcbg * 64) + (size_t)(fks * 192 + 32 * wi + 8 * g) * FF + e * 8;
#pragma unroll
        for (int i = 0; i < 8; ++i) fw[i] = __builtin_nontemporal_load((const GAS v4u*)(uintptr_t)(fp + (size_t)i * FF));
      }
      SB();
      ld_fix<PRE, KF>(kr, rk, ko);
      LOAD_EPOCH();
      if (w < 4) __builtin_amdgcn_s_setprio(2); else __builtin_amdgcn_s_setprio(1);
      PIN_EPOCH();
      __builtin_amdgcn_s_waitcnt(vmcnt_imm(2 * (KF - PRE)));
      SB();
      SB();
      if (isF) {
        v4f d = {0, 0, 0, 0};
        const unsigned xx[4] = {xf.x, xf.y, xf.z, xf.w};
#pragma unroll
        for (int i = 0; i < 4; ++i) { d = mfma(dm & dup_lo(xx[i]), fw[2 * i], d); d = mfma(dm & dup_hi(xx[i]), fw[2 * i + 1], d); }
        *(v4f*)&L.fu[wi][c * 16 + 4 * g] = d;
        lds_inc(&L.fcnt, l);
        STAMP(10);
      }
      SB();
    }
    ld_fix<KF, NPMIN>(kr, rk, ko);
    ld_opt(kr, rk, ko, np);
    STAMP(1);
    asm volatile("" : "+s"(np));
    ld_opt(vr, rv, l * 16, np);
    ld_fix<0, NPMIN>(vr, rv, l * 16);
    __builtin_amdgcn_s_setprio(3);
    if (w > 4) lds_set(&L.vflag[w - 5]);
    SB();
    STAMP(2);
    lds_wait(&L.qflag, 1);
    STAMP(3);
    asm volatile("" : "+s"(np));
    {
      const v4u qv = *(const v4u*)&L.qs[(l >> 2) & 1][16 * g + 4 * (l & 3)];
      const v4u zz = {0, 0, 0, 0};
      const v4u qlo = hb ? zz : qv, qhi = hb ? qv : zz;
      unsigned m0 = (l & 1) ? ~0u : 0u, m1 = (l & 2) ? ~0u : 0u;
      asm volatile("" : "+v"(m0), "+v"(m1));
      float sel[NP / 4];
#pragma unroll
      for (int G0 = 1; G0 <= NP / 4; ++G0) {
        const int G = G0 % (NP / 4);
        float d[4][4];
#pragma unroll
        for (int p = 0; p < 4; ++p) {
          v4f t = {0, 0, 0, 0};
          t = mfma(qlo, kr[8 * G + 2 * p], t);
          t = mfma(qhi, kr[8 * G + 2 * p + 1], t);
          d[p][0] = t.x; d[p][1] = t.y; d[p][2] = t.z; d[p][3] = t.w;
        }
        float xx[2];
#pragma unroll
        for (int h2 = 0; h2 < 2; ++h2) {
          const float* A = d[2 * h2]; const float* B = d[2 * h2 + 1];
          const float keep = bsel(m1, bsel(m0, B[3], A[2]), bsel(m0, B[1], A[0]));
          const float send = bsel(m1, bsel(m0, A[3], B[2]), bsel(m0, A[1], B[0]));
          xx[h2] = keep + dpp<DPP_X1>(send);
        }
        const float sv = bsel(m1, xx[1], xx[0]) + dpp<DPP_X2>(bsel(m1, xx[0], xx[1]));
        const int rp = 4 * G + (l & 3);
        sel[G] = (4 * G >= NOPT || rp >= NOPT || rp + NPMIN < np) ? sv * SCL : -INFINITY;
      }
      float mx = sel[0];
#pragma unroll
      for (int G = 1; G < NP / 4; ++G) mx = fmaxf(mx, sel[G]);
      mx = fmaxf(mx, dpp<DPP_X1>(mx)); mx = fmaxf(mx, dpp<DPP_X2>(mx)); mx = fmaxf(mx, dpp<DPP_ROR4>(mx)); mx = fmaxf(mx, dpp<DPP_ROR8>(mx));
      mx = swap32_max(mx);
      mx = as_f(as_u(mx) & 0xffffff00u);
      float ls = 0.f;
#pragma unroll
      for (int G = 0; G < NP / 4; ++G) {
        const float p = __builtin_amdgcn_exp2f(sel[G] - mx);
        ls += p;
        L.P[wi][(l >> 2) & 7][8 * G + 2 * (l & 3) + (l >> 5)] = pk_bf16(p, p);
      }
      ls += dpp<DPP_X1>(ls); ls += dpp<DPP_X2>(ls); ls += dpp<DPP_ROR4>(ls); ls += dpp<DPP_ROR8>(ls);
      ls = swap32_sum(ls);
      if ((l & 47) == 0) { v2f t = {mx, ls}; *(v2f*)&L.ml[wi][2 * g] = t; }
      lds_inc_nw(&L.mcnt, l);
    }
    SB();
    STAMP(6);
    {
      v4f o0 = {0, 0, 0, 0}, o1 = {0, 0, 0, 0};
#pragma unroll
      for (int k = 0; k < NP / 2; ++k) {
        const v4u pv = *(const v4u*)&L.P[wi][hb * 4 + g][4 * k];
        const unsigned p0 = pv.x, p1 = pv.y, p2 = pv.z, p3 = pv.w;
        o0 = mfma(dm & p0, vr[4 * k], o0); o1 = mfma(dm & p1, vr[4 * k + 1], o1);
        o0 = mfma(dm & p2, vr[4 * k + 2], o0); o1 = mfma(dm & p3, vr[4 * k + 3], o1);
      }
      *(v4f*)&L.ow[wi][(l >> 5) * 128 + 8 * c + 4 * (g & 1)] = o0 + o1;
    }
    lds_inc_nw(w < 4 ? &L.acnt : &L.ocnt, l);
    STAMP(4);
    if (w < 4) {
      if (isT) {
        const int cbk = 3 * cg + w - 1;
        const rsrc_t ro = mk_rsrc(wo + (size_t)R * 64 * DM, 64 * DM * 2);
        const rsrc_t rd = mk_rsrc(wd + (size_t)R * 72 * DM, 72 * DM * 2);
        const unsigned to = (2 * g + hb) * 1152 + cbk * 128 + e * 16;
        lds_wait(&L.vflag[w - 1], 1);
        __builtin_amdgcn_s_setprio(0);
        v4u tw[17];
#pragma unroll
        for (int i = 0; i < 9; ++i) tw[i] = ld_nt(rd, to, i * 9216);
#pragma unroll
        for (int i = 0; i < 8; ++i) tw[9 + i] = ld_nt(ro, to, i * 9216);
        SB();
        __builtin_amdgcn_s_setprio(3);
        STAMP(5);
        lds_wait(&L.hflag, 1);
        v4f d0 = {0, 0, 0, 0}, d1 = {0, 0, 0, 0};
        {
          const v4u h0 = *(const v4u*)&L.tabh[hb][g][0], h1 = *(const v4u*)&L.tabh[hb][g][4];
          const unsigned hp[9] = {h0.x, h0.y, h0.z, h0.w, h1.x, h1.y, h1.z, h1.w, L.tabh[hb][g][8]};
#pragma unroll
          for (int i = 0; i < 9; ++i) { d0 = mfma(dm & dup_lo(hp[i]), tw[i], d0); d1 = mfma(dm & dup_hi(hp[i]), tw[i], d1); }
        }
        v2f ma = {(e >> 1) == 0 ? 1.f : 0.f, (e >> 1) == 1 ? 1.f : 0.f}, mb = {(e >> 1) == 2 ? 1.f : 0.f, (e >> 1) == 3 ? 1.f : 0.f};
        unsigned tq = lds_addr(&L.tabo[0][e & 1][hb][g][0]);
        unsigned fo = WS_FB + (cbk * 64 + R) * 1024 + l * 16;
        v4u gf = {0, tag.t32, 0, tag.t32};
        unsigned t8v = tag.t8;
        asm volatile("" : "+v"(ma), "+v"(mb), "+v"(tq), "+v"(fo), "+{v[176:179]}"(d0), "+{v[180:183]}"(d1), "+{v[184:187]}"(gf), "+v"(t8v) : "v"(tw[16]));
        SB();
        STAMP(11);
        wg_barrier();
        STAMP(7);
        {
          asm volatile(
              "ds_read_b128 v[128:131], %[tq] offset:0\n\t"
              "ds_read_b128 v[132:135], %[tq] offset:512\n\t"
              "ds_read_b128 v[136:139], %[tq] offset:256\n\t"
              "ds_read_b128 v[140:143], %[tq] offset:768\n\t"
              "s_waitcnt lgkmcnt(3)\n\t"
              "v_pk_mul_f32 v[144:145], %[ma], v[128:129] op_sel_hi:[1,0]\n\t"
              "v_pk_mul_f32 v[146:147], %[mb], v[128:129] op_sel_hi:[1,0]\n\t"
              "s_waitcnt lgkmcnt(2)\n\t"
              "v_pk_mul_f32 v[148:149], %[ma], v[132:133] op_sel_hi:[1,0]\n\t"
              "v_pk_mul_f32 v[150:151], %[mb], v[132:133] op_sel_hi:[1,0]\n\t"
              "v_pk_mul_f32 v[152:153], %[ma], v[128:129] op_sel:[0,1]\n\t"
              "v_pk_mul_f32 v[154:155], %[mb], v[128:129] op_sel:[0,1]\n\t"
              "v_mfma_f32_16x16x32_bf16 %[d0], v[144:147], %[w0], %[d0]\n\t"
              "v_pk_mul_f32 v[156:157], %[ma], v[132:133] op_sel:[0,1]\n\t"
              "v_pk_mul_f32 v[158:159], %[mb], v[132:133] op_sel:[0,1]\n\t"
              "v_mfma_f32_16x16x32_bf16 %[d1], v[148:151], %[w0], %[d1]\n\t"
              "v_pk_mul_f32 v[160:161], %[ma], v[130:131] op_sel_hi:[1,0]\n\t"
              "v_pk_mul_f32 v[162:163], %[mb], v[130:131] op_sel_hi:[1,0]\n\t"
              "v_mfma_f32_16x16x32_bf16 %[d0], v[152:155], %[w1], %[d0]\n\t"
              "v_pk_mul_f32 v[164:165], %[ma], v[134:135] op_sel_hi:[1,0]\n\t"
              "v_pk_mul_f32 v[166:167], %[mb], v[134:135] op_sel_hi:[1,0]\n\t"
              "v_mfma_f32_16x16x32_bf16 %[d1], v[156:159], %[w1], %[d1]\n\t"
              "v_pk_mul_f32 v[168:169], %[ma], v[130:131] op_sel:[0,1]\n\t"
              "v_pk_mul_f32 v[170:171], %[mb], v[130:131] op_sel:[0,1]\n\t"
              "v_mfma_f32_16x16x32_bf16 %[d0], v[160:163], %[w2], %[d0]\n\t"
              "v_pk_mul_f32 v[172:173], %[ma], v[134:135] op_sel:[0,1]\n\t"
              "v_pk_mul_f32 v[174:175], %[mb], v[134:135] op_sel:[0,1]\n\t"
              "v_mfma_f32_16x16x32_bf16 %[d1], v[164:167], %[w2], %[d1]\n\t"
              "s_waitcnt lgkmcnt(1)\n\t"
              "v_pk_mul_f32 v[144:145], %[ma], v[136:137] op_sel_hi:[1,0]\n\t"
              "v_pk_mul_f32 v[146:147], %[mb], v[136:137] op_sel_hi:[1,0]\n\t"
              "v_mfma_f32_16x16x32_bf16 %[d0], v[168:171], %[w3], %[d0]\n\t"
              "s_waitcnt lgkmcnt(0)\n\t"
              "v_pk_mul_f32 v[148:149], %[ma], v[140:141] op_sel_hi:[1,0]\n\t"
              "v_pk_mul_f32 v[150:151], %[mb], v[140:141] op_sel_hi:[1,0]\n\t"
              "v_mfma_f32_16x16x32_bf16 %[d1], v[172:175], %[w3], %[d1]\n\t"
              "v_pk_mul_f32 v[152:153], %[ma], v[136:137] op_sel:[0,1]\n\t"
              "v_pk_mul_f32 v[154:155], %[mb], v[136:137] op_sel:[0,1]\n\t"
              "v_mfma_f32_16x16x32_bf16 %[d0], v[144:147], %[w4], %[d0]\n\t"
              "v_pk_mul_f32 v[156:157], %[ma], v[140:141] op_sel:[0,1]\n\t"
              "v_pk_mul_f32 v[158:159], %[mb], v[140:141] op_sel:[0,1]\n\t"
              "v_mfma_f32_16x16x32_bf16 %[d1], v[148:151], %[w4], %[d1]\n\t"
              "v_pk_mul_f32 v[160:161], %[ma], v[138:139] op_sel_hi:[1,0]\n\t"
              "v_pk_mul_f32 v[162:163], %[mb], v[138:139] op_sel_hi:[1,0]\n\t"
              "v_mfma_f32_16x16x32_bf16 %[d0], v[152:155], %[w5], %[d0]\n\t"
              "v_pk_mul_f32 v[164:165], %[ma], v[142:143] op_sel_hi:[1,0]\n\t"
              "v_pk_mul_f32 v[166:167], %[mb], v[142:143] op_sel_hi:[1,0]\n\t"
              "v_mfma_f32_16x16x32_bf16 %[d1], v[156:159], %[w5], %[d1]\n\t"
              "v_pk_mul_f32 v[168:169], %[ma], v[138:139] op_sel:[0,1]\n\t"
              "v_pk_mul_f32 v[170:171], %[mb], v[138:139] op_sel:[0,1]\n\t"
              "v_mfma_f32_16x16x32_bf16 %[d0], v[160:163], %[w6], %[d0]\n\t"
              "v_pk_mul_f32 v[172:173], %[ma], v[142:143] op_sel:[0,1]\n\t"
              "v_pk_mul_f32 v[174:175], %[mb], v[142:143] op_sel:[0,1]\n\t"
              "v_mfma_f32_16x16x32_bf16 %[d1], v[164:167], %[w6], %[d1]\n\t"
              "v_mfma_f32_16x16x32_bf16 %[d0], v[168:171], %[w7], %[d0]\n\t"
              "v_mfma_f32_16x16x32_bf16 %[d1], v[172:175], %[w7], %[d1]\n\t"
              "s_nop 7\n\t"
              "v_add_f32_dpp v128, v180, v176 row_ror:8 row_mask:0xf bank_mask:0x3\n\t"
              "v_add_f32_dpp v129, v181, v177 row_ror:8 row_mask:0xf bank_mask:0x3\n\t"
              "v_add_f32_dpp v128, v178, v182 row_ror:8 row_mask:0xf bank_mask:0xc\n\t"
              "v_add_f32_dpp v129, v179, v183 row_ror:8 row_mask:0xf bank_mask:0xc\n\t"
              "v_perm_b32 v184, v128, %[t8], %[ps]\n\t"
              "v_perm_b32 v186, v129, %[t8], %[ps]\n\t"
              "buffer_store_dwordx4 v[184:187], %[fo], %[srd], 0 offen sc1"
              : [d0] "+{v[176:179]}"(d0), [d1] "+{v[180:183]}"(d1), "+{v[184:187]}"(gf)
              : [tq] "v"(tq), [ma] "v"(ma), [mb] "v"(mb), [w0] "v"(tw[9]), [w1] "v"(tw[10]), [w2] "v"(tw[11]), [w3] "v"(tw[12]), [w4] "v"(tw[13]), [w5] "v"(tw[14]), [w6] "v"(tw[15]), [w7] "v"(tw[16]),
                [t8] "v"(t8v), [ps] "s"(0x07060500u), [fo] "v"(fo), [srd] "s"(srd)
              : "memory", "v128", "v129", "v130", "v131", "v132", "v133", "v134", "v135", "v136", "v137", "v138", "v139", "v140", "v141", "v142", "v143", "v144", "v145", "v146", "v147", "v148", "v149", "v150", "v151", "v152", "v153", "v154", "v155", "v156", "v157", "v158", "v159", "v160", "v161", "v162", "v163", "v164", "v165", "v166", "v167", "v168", "v169", "v170", "v171", "v172", "v173", "v174", "v175");
        }
        STAMP(8);
      } else {
        const int nt = (kvh * 2 + b) * 2 + (s & 1);
        const int rw = w < 3 ? (xg == 0 ? 2 : (xg == 3 ? 3 : xg - 1)) * 32 + (nt & 15) * 2 + (w - 1) : 128 + nt;
        if (rw < 144) {
          __builtin_amdgcn_s_setprio(0);
          lds_wait(&L.pvflag, 1);
          STAMP(5);
          const unsigned off = WS_FB + (rw >> 4) * 65536 + (l >> 2) * 4096 + (rw & 15) * 64 + (l & 3) * 16;
#pragma nounroll
          for (int spin = 0; spin < SPIN_MAX; ++spin) {
            CBAR();
            const v4u a = ld_sc(rws, off, 0);
            if (__builtin_amdgcn_ballot_w64(gbad(a, tag) == 0) != 0) break;
            __builtin_amdgcn_s_sleep(4);
          }
          const int sg = (rw & 15) * 4 + (l & 3);
          u64 op = (u64)(uintptr_t)(out + (sg >> 5) * DM + (rw >> 4) * 64 + 8 * (sg & 7) + 4 * ((sg >> 4) & 1) + 2 * ((sg >> 3) & 1));
          asm volatile("" : "+v"(op));
          v4u A0, A1, A2, A3, B0, B1, B2, B3, C0, C1, C2, C3;
          float v0, v1, p0, p1, q0, q1;
          unsigned e8, e32, u8, u32, cnt;
          u64 pm, sv;
          asm volatile(
              "s_mov_b64 %[sv], exec\n\ts_mov_b64 %[pm], exec\n\ts_mov_b32 %[cnt], %[lim]\n\t"
              "buffer_load_dwordx4 v[128:131], %[o0], %[srd], 0 offen offset:0 sc1\n\t"
              "buffer_load_dwordx4 v[132:135], %[o0], %[srd], 0 offen offset:1024 sc1\n\t"
              "buffer_load_dwordx4 v[136:139], %[o0], %[srd], 0 offen offset:2048 sc1\n\t"
              "buffer_load_dwordx4 v[140:143], %[o0], %[srd], 0 offen offset:3072 sc1\n\t"
              "s_sleep %[gap]\n\t"
              "buffer_load_dwordx4 v[144:147], %[o0], %[srd], 0 offen offset:0 sc1\n\t"
              "buffer_load_dwordx4 v[148:151], %[o0], %[srd], 0 offen offset:1024 sc1\n\t"
              "buffer_load_dwordx4 v[152:155], %[o0], %[srd], 0 offen offset:2048 sc1\n\t"
              "buffer_load_dwordx4 v[156:159], %[o0], %[srd], 0 offen offset:3072 sc1\n\t"
              "s_sleep %[gap]\n\t"
              "buffer_load_dwordx4 v[160:163], %[o0], %[srd], 0 offen offset:0 sc1\n\t"
              "buffer_load_dwordx4 v[164:167], %[o0], %[srd], 0 offen offset:1024 sc1\n\t"
              "buffer_load_dwordx4 v[168:171], %[o0], %[srd], 0 offen offset:2048 sc1\n\t"
              "buffer_load_dwordx4 v[172:175], %[o0], %[srd], 0 offen offset:3072 sc1\n\t"
              ".Lrd_loop%=:\n\t"
              "s_waitcnt vmcnt(11)\n\t"
              "v_bitop3_b32 %[e8], v128, v130, %[t8] bitop3:0x7e\n\t"
              "v_bitop3_b32 %[e32], v129, v131, %[t32] bitop3:0x7e\n\t"
              "v_and_b32 %[p0], %[msk], v128\n\t"
              "v_and_b32 %[p1], %[msk], v130\n\t"
              "s_waitcnt vmcnt(10)\n\t"
              "v_bitop3_b32 %[u8], v132, v134, %[t8] bitop3:0x7e\n\t"
              "v_bitop3_b32 %[u32], v133, v135, %[t32] bitop3:0x7e\n\t"
              "v_and_b32 %[q0], %[msk], v132\n\t"
              "v_and_b32 %[q1], %[msk], v134\n\t"
              "v_or_b32 %[e8], %[e8], %[u8]\n\t"
              "v_or_b32 %[e32], %[e32], %[u32]\n\t"
              "v_add_f32 %[p0], %[p0], %[q0]\n\t"
              "v_add_f32 %[p1], %[p1], %[q1]\n\t"
              "s_waitcnt vmcnt(9)\n\t"
              "v_bitop3_b32 %[u8], v136, v138, %[t8] bitop3:0x7e\n\t"
              "v_bitop3_b32 %[u32], v137, v139, %[t32] bitop3:0x7e\n\t"
              "v_and_b32 %[q0], %[msk], v136\n\t"
              "v_and_b32 %[q1], %[msk], v138\n\t"
              "v_or_b32 %[e8], %[e8], %[u8]\n\t"
              "v_or_b32 %[e32], %[e32], %[u32]\n\t"
              "v_add_f32 %[p0], %[p0], %[q0]\n\t"
              "v_add_f32 %[p1], %[p1], %[q1]\n\t"
              "s_waitcnt vmcnt(8)\n\t"
              "v_bitop3_b32 %[u8], v140, v142, %[t8] bitop3:0x7e\n\t"
              "v_bitop3_b32 %[u32], v141, v143, %[t32] bitop3:0x7e\n\t"
              "v_bitop3_b32 %[e8], %[e8], %[u8], %[ff] bitop3:0xa8\n\t"
              "v_or3_b32 %[e8], %[e8], %[e32], %[u32]\n\t"
              "v_cmp_eq_u32 vcc, 0, %[e8]\n\t"
              "v_and_b32 %[q0], %[msk], v140\n\t"
              "v_and_b32 %[q1], %[msk], v142\n\t"
              "s_and_b64 exec, vcc, %[pm]\n\t"
              "v_add_f32 %[v0], %[p0], %[q0]\n\t"
              "v_add_f32 %[v1], %[p1], %[q1]\n\t"
              "s_andn2_b64 %[pm], %[pm], exec\n\t"
              "s_cbranch_scc0 .Lrd_done%=\n\t"
              "s_bcnt1_i32_b64 vcc_lo, %[pm]\n\t"
              "s_cmp_le_u32 vcc_lo, %[slow]\n\t"
              "s_cbranch_scc1 .Lrd_fast0_%=\n\t"
              "s_sleep %[gap]\n\t"
              ".Lrd_fast0_%=:\n\t"
              "s_mov_b64 exec, %[pm]\n\t"
              "buffer_load_dwordx4 v[128:131], %[o0], %[srd], 0 offen offset:0 sc1\n\t"
              "buffer_load_dwordx4 v[132:135], %[o0], %[srd], 0 offen offset:1024 sc1\n\t"
              "buffer_load_dwordx4 v[136:139], %[o0], %[srd], 0 offen offset:2048 sc1\n\t"
              "buffer_load_dwordx4 v[140:143], %[o0], %[srd], 0 offen offset:3072 sc1\n\t"
              "s_mov_b64 exec, %[sv]\n\t"
              "s_waitcnt vmcnt(11)\n\t"
              "v_bitop3_b32 %[e8], v144, v146, %[t8] bitop3:0x7e\n\t"
              "v_bitop3_b32 %[e32], v145, v147, %[t32] bitop3:0x7e\n\t"
              "v_and_b32 %[p0], %[msk], v144\n\t"
              "v_and_b32 %[p1], %[msk], v146\n\t"
              "s_waitcnt vmcnt(10)\n\t"
              "v_bitop3_b32 %[u8], v148, v150, %[t8] bitop3:0x7e\n\t"
              "v_bitop3_b32 %[u32], v149, v151, %[t32] bitop3:0x7e\n\t"
              "v_and_b32 %[q0], %[msk], v148\n\t"
              "v_and_b32 %[q1], %[msk], v150\n\t"
              "v_or_b32 %[e8], %[e8], %[u8]\n\t"
              "v_or_b32 %[e32], %[e32], %[u32]\n\t"
              "v_add_f32 %[p0], %[p0], %[q0]\n\t"
              "v_add_f32 %[p1], %[p1], %[q1]\n\t"
              "s_waitcnt vmcnt(9)\n\t"
              "v_bitop3_b32 %[u8], v152, v154, %[t8] bitop3:0x7e\n\t"
              "v_bitop3_b32 %[u32], v153, v155, %[t32] bitop3:0x7e\n\t"
              "v_and_b32 %[q0], %[msk], v152\n\t"
              "v_and_b32 %[q1], %[msk], v154\n\t"
              "v_or_b32 %[e8], %[e8], %[u8]\n\t"
              "v_or_b32 %[e32], %[e32], %[u32]\n\t"
              "v_add_f32 %[p0], %[p0], %[q0]\n\t"
              "v_add_f32 %[p1], %[p1], %[q1]\n\t"
              "s_waitcnt vmcnt(8)\n\t"
              "v_bitop3_b32 %[u8], v156, v158, %[t8] bitop3:0x7e\n\t"
              "v_bitop3_b32 %[u32], v157, v159, %[t32] bitop3:0x7e\n\t"
              "v_bitop3_b32 %[e8], %[e8], %[u8], %[ff] bitop3:0xa8\n\t"
              "v_or3_b32 %[e8], %[e8], %[e32], %[u32]\n\t"
              "v_cmp_eq_u32 vcc, 0, %[e8]\n\t"
              "v_and_b32 %[q0], %[msk], v156\n\t"
              "v_and_b32 %[q1], %[msk], v158\n\t"
              "s_and_b64 exec, vcc, %[pm]\n\t"
              "v_add_f32 %[v0], %[p0], %[q0]\n\t"
              "v_add_f32 %[v1], %[p1], %[q1]\n\t"
              "s_andn2_b64 %[pm], %[pm], exec\n\t"
              "s_cbranch_scc0 .Lrd_done%=\n\t"
              "s_bcnt1_i32_b64 vcc_lo, %[pm]\n\t"
              "s_cmp_le_u32 vcc_lo, %[slow]\n\t"
              "s_cbranch_scc1 .Lrd_fast1_%=\n\t"
              "s_sleep %[gap]\n\t"
              ".Lrd_fast1_%=:\n\t"
              "s_mov_b64 exec, %[pm]\n\t"
              "buffer_load_dwordx4 v[144:147], %[o0], %[srd], 0 offen offset:0 sc1\n\t"
              "buffer_load_dwordx4 v[148:151], %[o0], %[srd], 0 offen offset:1024 sc1\n\t"
              "buffer_load_dwordx4 v[152:155], %[o0], %[srd], 0 offen offset:2048 sc1\n\t"
              "buffer_load_dwordx4 v[156:159], %[o0], %[srd], 0 offen offset:3072 sc1\n\t"
              "s_mov_b64 exec, %[sv]\n\t"
              "s_waitcnt vmcnt(11)\n\t"
              "v_bitop3_b32 %[e8], v160, v162, %[t8] bitop3:0x7e\n\t"
              "v_bitop3_b32 %[e32], v161, v163, %[t32] bitop3:0x7e\n\t"
              "v_and_b32 %[p0], %[msk], v160\n\t"
              "v_and_b32 %[p1], %[msk], v162\n\t"
              "s_waitcnt vmcnt(10)\n\t"
              "v_bitop3_b32 %[u8], v164, v166, %[t8] bitop3:0x7e\n\t"
              "v_bitop3_b32 %[u32], v165, v167, %[t32] bitop3:0x7e\n\t"
              "v_and_b32 %[q0], %[msk], v164\n\t"
              "v_and_b32 %[q1], %[msk], v166\n\t"
              "v_or_b32 %[e8], %[e8], %[u8]\n\t"
              "v_or_b32 %[e32], %[e32], %[u32]\n\t"
              "v_add_f32 %[p0], %[p0], %[q0]\n\t"
              "v_add_f32 %[p1], %[p1], %[q1]\n\t"
              "s_waitcnt vmcnt(9)\n\t"
              "v_bitop3_b32 %[u8], v168, v170, %[t8] bitop3:0x7e\n\t"
              "v_bitop3_b32 %[u32], v169, v171, %[t32] bitop3:0x7e\n\t"
              "v_and_b32 %[q0], %[msk], v168\n\t"
              "v_and_b32 %[q1], %[msk], v170\n\t"
              "v_or_b32 %[e8], %[e8], %[u8]\n\t"
              "v_or_b32 %[e32], %[e32], %[u32]\n\t"
              "v_add_f32 %[p0], %[p0], %[q0]\n\t"
              "v_add_f32 %[p1], %[p1], %[q1]\n\t"
              "s_waitcnt vmcnt(8)\n\t"
              "v_bitop3_b32 %[u8], v172, v174, %[t8] bitop3:0x7e\n\t"
              "v_bitop3_b32 %[u32], v173, v175, %[t32] bitop3:0x7e\n\t"
              "v_bitop3_b32 %[e8], %[e8], %[u8], %[ff] bitop3:0xa8\n\t"
              "v_or3_b32 %[e8], %[e8], %[e32], %[u32]\n\t"
              "v_cmp_eq_u32 vcc, 0, %[e8]\n\t"
              "v_and_b32 %[q0], %[msk], v172\n\t"
              "v_and_b32 %[q1], %[msk], v174\n\t"
              "s_and_b64 exec, vcc, %[pm]\n\t"
              "v_add_f32 %[v0], %[p0], %[q0]\n\t"
              "v_add_f32 %[v1], %[p1], %[q1]\n\t"
              "s_andn2_b64 %[pm], %[pm], exec\n\t"
              "s_cbranch_scc0 .Lrd_done%=\n\t"
              "s_bcnt1_i32_b64 vcc_lo, %[pm]\n\t"
              "s_cmp_le_u32 vcc_lo, %[slow]\n\t"
              "s_cbranch_scc1 .Lrd_fast2_%=\n\t"
              "s_sleep %[gap]\n\t"
              ".Lrd_fast2_%=:\n\t"
              "s_mov_b64 exec, %[pm]\n\t"
              "buffer_load_dwordx4 v[160:163], %[o0], %[srd], 0 offen offset:0 sc1\n\t"
              "buffer_load_dwordx4 v[164:167], %[o0], %[srd], 0 offen offset:1024 sc1\n\t"
              "buffer_load_dwordx4 v[168:171], %[o0], %[srd], 0 offen offset:2048 sc1\n\t"
              "buffer_load_dwordx4 v[172:175], %[o0], %[srd], 0 offen offset:3072 sc1\n\t"
              "s_mov_b64 exec, %[sv]\n\t"
              "s_sub_u32 %[cnt], %[cnt], 1\n\ts_cbranch_scc0 .Lrd_loop%=\n\t"
              ".Lrd_done%=:\n\ts_mov_b64 exec, %[sv]\n\ts_nop 1"
              : "=&{v[128:131]}"(A0), "=&{v[132:135]}"(A1), "=&{v[136:139]}"(A2), "=&{v[140:143]}"(A3), "=&{v[144:147]}"(B0), "=&{v[148:151]}"(B1), "=&{v[152:155]}"(B2), "=&{v[156:159]}"(B3), "=&{v[160:163]}"(C0), "=&{v[164:167]}"(C1), "=&{v[168:171]}"(C2), "=&{v[172:175]}"(C3),
                [v0] "=&v"(v0), [v1] "=&v"(v1), [p0] "=&v"(p0), [p1] "=&v"(p1), [q0] "=&v"(q0), [q1] "=&v"(q1),
                [e8] "=&v"(e8), [e32] "=&v"(e32), [u8] "=&v"(u8), [u32] "=&v"(u32), [pm] "=&s"(pm), [sv] "=&s"(sv), [cnt] "=&s"(cnt)
              : [o0] "v"(off), [srd] "s"(srd), [t32] "s"(tag.t32), [t8] "s"(tag.t8), [ff] "s"(0xffu), [msk] "s"(0xffffff00u),
                [lim] "i"(SPIN_MAX), [gap] "i"(RGAP), [slow] "i"(RSLOW)
              : "vcc", "scc", "memory");
          STAMP(8);
          v0 += dpp<DPP_ROR4>(v0); v1 += dpp<DPP_ROR4>(v1);
          v0 += dpp<DPP_ROR8>(v0); v1 += dpp<DPP_ROR8>(v1);
          v0 = sum_rows(v0); v1 = sum_rows(v1);
          if (l < 4) {
            *(GAS unsigned*)op = pk_bf16(v0, v1);
            if (rw == 0 && l == 0) *(GAS u64*)(uintptr_t)(ws + WS_EPOCH) = ep;
          }
          asm volatile("s_waitcnt vmcnt(0)" :: "{v[128:131]}"(A0), "{v[132:135]}"(A1), "{v[136:139]}"(A2), "{v[140:143]}"(A3), "{v[144:147]}"(B0), "{v[148:151]}"(B1), "{v[152:155]}"(B2), "{v[156:159]}"(B3), "{v[160:163]}"(C0), "{v[164:167]}"(C1), "{v[168:171]}"(C2), "{v[172:175]}"(C3));
        }
      }
    } else if (isT) {
      lds_wait(&L.pvflag, 1);
      o_gather(w - 4);
    }
  }
  STAMP(9);
}

extern "C" void kernel_launch(void* const* d_in, const int* in_sizes, int n_in, void* d_out, int out_size, void* d_ws, size_t ws_size, hipStream_t stream) {
  fused_decode<<<256, 512, 0, stream>>>((const u16*)d_in[0], (const u16*)d_in[1], (const u16*)d_in[2], (const u16*)d_in[3], (const u16*)d_in[4],
                                        (const u16*)d_in[6], (const u16*)d_in[7], (const u16*)d_in[8], (const u16*)d_in[9], (u16*)d_out,
                                        (unsigned char*)d_ws);
}
```
